```python
import torch

def _softmax(x, axis=-1):
    return torch.softmax(x, dim=axis)
def _gelu(x):
    return torch.nn.functional.gelu(x, approximate="tanh")
def _silu(x):
    return torch.nn.functional.silu(x)
def _sigmoid(x):
    return torch.sigmoid(x)
def _layernorm(x, axis=-1, eps=1e-5):
    m = x.mean(dim=axis, keepdim=True)
    v = x.var(dim=axis, keepdim=True, unbiased=False)
    return (x - m) / torch.sqrt(v + eps)
def _scatter_add(tgt, idx, src, axis=0):
    return tgt.clone().index_add_(axis, idx, src)
def _segment_sum(src, seg, n):
    out = torch.zeros((n,) + src.shape[1:], dtype=src.dtype)
    return out.index_add_(0, seg, src)
def _pad(x, pw):
    flat = tuple(v for pair in reversed(pw) for v in pair)
    return torch.nn.functional.pad(x, flat)

kernel_name = 'gather_scatter__cheap_equivariant_egnn__graph'

def setup_inputs():

    _w = (lambda a: (lambda t: t.to(torch.bfloat16) if t.is_floating_point() else t)(torch.as_tensor(a)))
    N, deg, D, De = 50000, 16, 64, max(16, 1)
    E = N * deg
    return {
        "h": _w((lambda *s: torch.randn(*s, dtype=torch.bfloat16) * 0.2)(N, D)),
        "pos": _w((lambda *s: torch.randn(*s, dtype=torch.bfloat16) * 0.8)(N, 3)),
        "e_attr": _w((lambda *s: torch.randn(*s, dtype=torch.bfloat16) * 0.8)(E, De)),
        "src": _w((lambda lo, hi, *s: torch.randint(lo, hi, s))(0, N, E).to(torch.int64)),
        "dst": _w((lambda lo, hi, *s: torch.randint(lo, hi, s))(0, N, E).to(torch.int64)),
        "w_e": _w(0.1 * (lambda *s: torch.randn(*s, dtype=torch.bfloat16) * 0.8)(2 * D + 1 + De, D)),
        "w_x": _w(0.1 * (lambda *s: torch.randn(*s, dtype=torch.bfloat16) * 0.2)(D, 1)),
        "w_h": _w(0.1 * (lambda *s: torch.randn(*s, dtype=torch.bfloat16) * 0.2)(2 * D, D)),
    }

def reference(h, pos, e_attr, src, dst, w_e, w_x, w_h):
    N, D = h.shape[0], h.shape[1]
    diff = pos[dst] - pos[src]
    dist2 = torch.sum(diff * diff, axis=-1, keepdims=True)
    e_in = torch.concatenate([h[dst], h[src], dist2, e_attr], axis=-1)
    m_ij = _silu(e_in @ w_e)
    coord_scale = m_ij @ w_x
    pos_new = pos + _segment_sum(diff * coord_scale, dst, N)
    m_i = _segment_sum(m_ij, dst, N)
    h_new = h + _silu(torch.concatenate([h, m_i], axis=-1) @ w_h)
    return h_new, pos_new
```

```cpp
#include <hip/hip_runtime.h>
#include <hip/hip_bf16.h>
#include <stdint.h>

static __device__ __forceinline__ float bf2f(uint16_t v) { return __uint_as_float(((uint32_t)v) << 16); }
static __device__ __forceinline__ uint16_t f2bf(float f) { __hip_bfloat16 b = __float2bfloat16(f); return *(uint16_t*)&b; }
static __device__ __forceinline__ float rbf(float f) { return bf2f(f2bf(f)); }

__global__ void k_zero(float* p, int n) { int i = blockIdx.x * 256 + threadIdx.x; if (i < n) p[i] = 0.f; }

__global__ void k_edge(const uint16_t* h, const uint16_t* pos, const uint16_t* ea, const int* src, const int* dst,
                       const uint16_t* we, const uint16_t* wx, float* mi, float* pa, int E) {
    int e = blockIdx.x * 4 + (threadIdx.x >> 6);
    int f = threadIdx.x & 63;
    if (e >= E) return;
    int s = src[e], d = dst[e];
    float df[3]; float d2 = 0.f;
    for (int c = 0; c < 3; ++c) { df[c] = rbf(bf2f(pos[d * 3 + c]) - bf2f(pos[s * 3 + c])); d2 += rbf(df[c] * df[c]); }
    d2 = rbf(d2);
    float acc = 0.f;
    for (int k = 0; k < 64; ++k) acc += bf2f(h[d * 64 + k]) * bf2f(we[k * 64 + f]);
    for (int k = 0; k < 64; ++k) acc += bf2f(h[s * 64 + k]) * bf2f(we[(64 + k) * 64 + f]);
    acc += d2 * bf2f(we[128 * 64 + f]);
    for (int k = 0; k < 16; ++k) acc += bf2f(ea[e * 16 + k]) * bf2f(we[(129 + k) * 64 + f]);
    float m = rbf(acc / (1.f + __expf(-acc)));
    float cs = m * bf2f(wx[f]);
    for (int o = 32; o > 0; o >>= 1) cs += __shfl_xor(cs, o, 64);
    cs = rbf(cs);
    atomicAdd(&mi[d * 64 + f], m);
    if (f < 3) atomicAdd(&pa[d * 3 + f], rbf(df[f] * cs));
}

__global__ void k_node(const uint16_t* h, const uint16_t* pos, const uint16_t* wh, const float* mi, const float* pa,
                       uint16_t* out, int N) {
    int n = blockIdx.x * 4 + (threadIdx.x >> 6);
    int f = threadIdx.x & 63;
    if (n >= N) return;
    float acc = 0.f;
    for (int k = 0; k < 64; ++k) acc += bf2f(h[n * 64 + k]) * bf2f(wh[k * 64 + f]);
    for (int k = 0; k < 64; ++k) acc += rbf(mi[n * 64 + k]) * bf2f(wh[(64 + k) * 64 + f]);
    float y = rbf(acc / (1.f + __expf(-acc)));
    out[n * 64 + f] = f2bf(bf2f(h[n * 64 + f]) + y);
    if (f < 3) out[(size_t)N * 64 + n * 3 + f] = f2bf(bf2f(pos[n * 3 + f]) + rbf(pa[n * 3 + f]));
}

extern "C" void kernel_launch(void* const* d_in, const int* in_sizes, int n_in,
                              void* d_out, int out_size, void* d_ws, size_t ws_size,
                              hipStream_t stream) {
    const uint16_t* h = (const uint16_t*)d_in[0];
    const uint16_t* pos = (const uint16_t*)d_in[1];
    const uint16_t* ea = (const uint16_t*)d_in[2];
    const int* src = (const int*)d_in[3];
    const int* dst = (const int*)d_in[4];
    const uint16_t* we = (const uint16_t*)d_in[5];
    const uint16_t* wx = (const uint16_t*)d_in[6];
    const uint16_t* wh = (const uint16_t*)d_in[7];
    int N = in_sizes[0] / 64, E = in_sizes[3];
    float* mi = (float*)d_ws; float* pa = mi + (size_t)N * 64;
    int nz = N * 67;
    k_zero<<<(nz + 255) / 256, 256, 0, stream>>>(mi, nz);
    k_edge<<<(E + 3) / 4, 256, 0, stream>>>(h, pos, ea, src, dst, we, wx, mi, pa, E);
    k_node<<<(N + 3) / 4, 256, 0, stream>>>(h, pos, wh, mi, pa, (uint16_t*)d_out, N);
}
```

```cpp
#include <hip/hip_runtime.h>
#include <hip/hip_bf16.h>
#include <stdint.h>

#define MOVE_EA 0

using u32x4 = __attribute__((ext_vector_type(4))) unsigned;
using u32x2 = __attribute__((ext_vector_type(2))) unsigned;
using f32x16 = __attribute__((ext_vector_type(16))) float;
using f32x4 = __attribute__((ext_vector_type(4))) float;
using f32x2 = __attribute__((ext_vector_type(2))) float;
using bf16x8 = __attribute__((ext_vector_type(8))) short;
using s16x4 = __attribute__((ext_vector_type(4))) short;
using bf16x2 = __attribute__((ext_vector_type(2))) __bf16;
#define DI __device__ __forceinline__
#define ACLOB "v240", "v241", "v242", "v243", "v244", "v245", "v246", "v247", "v248", "v249", "v250", "v251", "v252", "v253", "v254", "v255"
#define MFMA32(a, b, c) __builtin_amdgcn_mfma_f32_32x32x16_bf16((a), (b), (c), 0, 0, 0)

constexpr int CHUNK = 3136;
constexpr int BINSZ = 196;
constexpr int NBIN = 256;
constexpr int NBLK = 256;
constexpr int PH = 8;
constexpr int NKEY = NBIN * PH;
constexpr int CAP = 3392;
constexpr int LISTN = CAP + 64;

constexpr int PROW = 288;
constexpr int L_P = 0;
constexpr int L_SRC = BINSZ * PROW;
constexpr int L_W1 = L_SRC + LISTN * 2;
constexpr int L_TAB = L_W1 + LISTN * 4;
constexpr int L_START = L_TAB + 160;
constexpr int L_STG = ((L_START + 264 * 4 + 127) / 128) * 128;
constexpr int STG_W = 10240;
constexpr int L_TOTAL = L_STG + 8 * STG_W;
constexpr int L_HIST = L_STG;
static_assert(L_TOTAL <= 163840, "LDS");

DI float bf2f(uint32_t v) { return __uint_as_float(v << 16); }
DI uint32_t pkbf(float a, float b) { bf16x2 v; v[0] = (__bf16)a; v[1] = (__bf16)b; return __builtin_bit_cast(uint32_t, v); }
DI uint16_t f2bf(float a) { __bf16 v = (__bf16)a; return __builtin_bit_cast(uint16_t, v); }

__global__ __launch_bounds__(1024) void k1_partition(const int* __restrict__ src, const int* __restrict__ dst, const u32x4* __restrict__ ea,
                                                     const uint16_t* __restrict__ pos, const uint16_t* __restrict__ we, const uint16_t* __restrict__ wx,
                                                     const uint16_t* __restrict__ wh,
                                                     uint32_t* __restrict__ R0, uint32_t* __restrict__ R1, uint32_t* __restrict__ T,
                                                     u32x4* __restrict__ EA2, f32x4* __restrict__ pos4, u32x4* __restrict__ WF, int N, int E) {
    extern __shared__ __attribute__((aligned(16))) char lds[];
    uint32_t* hist = (uint32_t*)lds;
    uint32_t* wsum = hist + NKEY;
    uint32_t* recS = wsum + 16;
    uint32_t* recE = recS + CHUNK;
    u32x4* eaS = (u32x4*)(recE + CHUNK);
    const int tid = threadIdx.x, blk = blockIdx.x;
    const int e0 = blk * CHUNK;
    const int cnt = min(CHUNK, E - e0);
    hist[tid] = 0; hist[tid + 1024] = 0;
    int d[4], s[4];
#pragma unroll
    for (int r = 0; r < 4; ++r) { int i = r * 1024 + tid; d[r] = i < cnt ? dst[e0 + i] : 0; s[r] = i < cnt ? src[e0 + i] : 0; }
    for (int n = blk * BINSZ + tid; tid < BINSZ && n < N; n += gridDim.x * BINSZ) {
        f32x4 p; p[0] = bf2f(pos[n * 3]); p[1] = bf2f(pos[n * 3 + 1]); p[2] = bf2f(pos[n * 3 + 2]); p[3] = 0.f; pos4[n] = p;
    }
    if (tid >= 960) {
        for (int f = blk; f < 40; f += gridDim.x) {
            const int l = tid - 960, r = l & 31, h = l >> 5;
            uint32_t o[4];
#pragma unroll
            for (int jj = 0; jj < 4; ++jj) {
                uint32_t e2[2];
#pragma unroll
                for (int u = 0; u < 2; ++u) {
                    const int j = 2 * jj + u;
                    const int kn = 8 * h + j, kp = 8 * (j >> 2) + 4 * h + (j & 3);
                    uint32_t val;
                    if (f < 8) { int fb = f >> 2, sx = f & 3; val = we[(64 + 16 * sx + kn) * 64 + 32 * fb + r]; }
                    else if (f < 10) { int fb = f - 8; val = we[(129 + kn) * 64 + 32 * fb + r]; }
                    else if (f < 12) { int fb = f - 10; val = (kn == 0) ? we[128 * 64 + 32 * fb + r] : 0; }
                    else if (f < 16) { int fb = (f - 12) >> 1, sx = (f - 12) & 1; val = wx[32 * fb + 16 * sx + kp]; }
                    else if (f < 24) { int fb = (f - 16) >> 2, sx = (f - 16) & 3; val = we[(16 * sx + kn) * 64 + 32 * fb + r]; }
                    else if (f < 32) { int ob = (f - 24) >> 2, sx = (f - 24) & 3; val = wh[(16 * sx + kn) * 64 + 32 * ob + r]; }
                    else { int ob = (f - 32) >> 2, fb = ((f - 32) >> 1) & 1, sx = (f - 32) & 1; val = wh[(64 + 32 * fb + 16 * sx + kp) * 64 + 32 * ob + r]; }
                    e2[u] = val;
                }
                o[jj] = e2[0] | (e2[1] << 16);
            }
            u32x4 ov; ov[0] = o[0]; ov[1] = o[1]; ov[2] = o[2]; ov[3] = o[3];
            WF[f * 64 + l] = ov;
        }
    }
    __syncthreads();
    uint32_t key[4], rank[4], rec[4];
#pragma unroll
    for (int r = 0; r < 4; ++r) {
        int i = r * 1024 + tid;
        uint32_t bin = (uint32_t)d[r] / BINSZ, dl = d[r] - bin * BINSZ;
        uint32_t ph = (dl * 2 % 49) * 8 / 49;
        key[r] = bin * PH + ph; rec[r] = (uint32_t)s[r] | (dl << 16);
        rank[r] = 0;
        if (i < cnt) rank[r] = atomicAdd(&hist[key[r]], 1u);
    }
    __syncthreads();
    uint32_t a = hist[2 * tid], b = hist[2 * tid + 1];
    uint32_t x = a + b;
    const int lane = tid & 63, w = tid >> 6;
#pragma unroll
    for (int o = 1; o < 64; o <<= 1) { uint32_t y = __shfl_up(x, o, 64); if (lane >= o) x += y; }
    if (lane == 63) wsum[w] = x;
    __syncthreads();
    uint32_t wp = 0;
#pragma unroll
    for (int i = 0; i < 16; ++i) wp += (i < w) ? wsum[i] : 0;
    uint32_t ex = wp + x - (a + b);
    hist[2 * tid] = ex; hist[2 * tid + 1] = ex + a;
    uint32_t incl = wp + x;
    uint32_t endv = __shfl_down(incl, 3, 64);
    if ((tid & 3) == 0) T[(tid >> 2) * NBLK + blk] = ex | ((endv - ex) << 16);
    __syncthreads();
#pragma unroll
    for (int r = 0; r < 4; ++r) {
        int i = r * 1024 + tid;
        if (i < cnt) {
            uint32_t p = hist[key[r]] + rank[r];
            recS[p] = rec[r];
            recE[p] = e0 + i;
        }
    }
    __syncthreads();
#pragma unroll
    for (int r = 0; r < 4; ++r) {
        int i = r * 1024 + tid;
        if (i < cnt) {
            R0[e0 + i] = recS[i];
            R1[e0 + i] = recE[i];
        }
    }
}

DI void dma16(uint32_t ldsaddr, uint32_t voff, const void* base) {
    asm volatile("s_mov_b32 m0, %0\n\ts_nop 0\n\tglobal_load_lds_dwordx4 %1, %2" :: "s"(ldsaddr), "v"(voff), "s"(base) : "memory");
}
typedef __attribute__((address_space(3))) s16x4* lds_s16x4_p;
DI s16x4 trread(uint32_t addr) { return __builtin_amdgcn_ds_read_tr16_b64_v4i16((lds_s16x4_p)addr); }

template <class T> DI T ldsld(uint32_t addr) { return *(__attribute__((address_space(3))) T*)addr; }
template <class T> DI void ldsst(uint32_t addr, T v) { *(__attribute__((address_space(3))) T*)addr = v; }

DI float silu1(float x) { return x * __builtin_amdgcn_rcpf(1.f + __builtin_amdgcn_exp2f(x * -1.44269504089f)); }

template <int V> struct IC { static constexpr int value = V; };

__global__ __launch_bounds__(512, 2) __attribute__((amdgpu_num_vgpr(240))) void k2_main(const uint16_t* __restrict__ hg, const char* __restrict__ eag, const char* __restrict__ pos4g,
                                                  const uint32_t* __restrict__ R0, const uint32_t* __restrict__ R1, const uint32_t* __restrict__ T,
                                                  const u32x4* __restrict__ WF, uint16_t* __restrict__ out, int N, int nblk) {
    extern __shared__ __attribute__((aligned(16))) char ldsraw[];
    const uint32_t LB = (uint32_t)(uintptr_t)(__attribute__((address_space(3))) char*)ldsraw;
    const int tid = threadIdx.x, lane = tid & 63;
    const int w = __builtin_amdgcn_readfirstlane(tid >> 6);
    const int e = lane & 31, h = lane >> 5;
    const int c = blockIdx.x;
    const int node0 = c * BINSZ;
    const int nnodes = min(BINSZ, N - node0);

    u32x4 T4 = *(const u32x4*)(T + (size_t)c * NBLK + 4 * lane);
#pragma unroll
    for (int i = 0; i < 4; ++i) if (4 * lane + i >= nblk) T4[i] = 0;

    if (w < 7) {
        const int nl = 32 * w + e;
        const int ng = min(node0 + nl, N - 1);
        bf16x8 hb[4];
#pragma unroll
        for (int s = 0; s < 4; ++s) hb[s] = *(const bf16x8*)(hg + (size_t)ng * 64 + 16 * s + 8 * h);
        f32x4 pd = *(const f32x4*)(pos4g + (size_t)ng * 16);
        f32x16 p0, p1;
#pragma unroll
        for (int i = 0; i < 16; ++i) { p0[i] = 0.f; p1[i] = 0.f; }
#pragma unroll
        for (int s = 0; s < 4; ++s) {
            p0 = MFMA32(__builtin_bit_cast(bf16x8, WF[(16 + s) * 64 + lane]), hb[s], p0);
            p1 = MFMA32(__builtin_bit_cast(bf16x8, WF[(20 + s) * 64 + lane]), hb[s], p1);
        }
        if (nl < BINSZ) {
            const uint32_t pa = LB + L_P + nl * PROW + 16 * h;
#pragma unroll
            for (int g = 0; g < 4; ++g) {
                f32x4 a, b;
#pragma unroll
                for (int i = 0; i < 4; ++i) { a[i] = p0[4 * g + i]; b[i] = p1[4 * g + i]; }
                ldsst<f32x4>(pa + 32 * g, a); ldsst<f32x4>(pa + 128 + 32 * g, b);
            }
            f32x4 z = {0.f, 0.f, 0.f, 0.f};
            ldsst<f32x4>(pa + 256, h == 0 ? pd : z);
        }
    } else if (lane < 36) {
        const int k = lane >> 2, q = lane & 3;
        uint32_t v = (2 * q < k ? 0x3F80u : 0u) | (2 * q + 1 < k ? 0x3F800000u : 0u);
        ldsst<uint32_t>(LB + L_TAB + lane * 4, v);
    }

    bf16x8 W2[8], Wa[2], Wd[2], Wx[4];
#pragma unroll
    for (int i = 0; i < 8; ++i) W2[i] = __builtin_bit_cast(bf16x8, WF[i * 64 + lane]);
#pragma unroll
    for (int i = 0; i < 2; ++i) { Wa[i] = __builtin_bit_cast(bf16x8, WF[(8 + i) * 64 + lane]); Wd[i] = __builtin_bit_cast(bf16x8, WF[(10 + i) * 64 + lane]); }
#pragma unroll
    for (int i = 0; i < 4; ++i) Wx[i] = __builtin_bit_cast(bf16x8, WF[(12 + i) * 64 + lane]);

    uint32_t tot = (T4[0] >> 16) + (T4[1] >> 16) + (T4[2] >> 16) + (T4[3] >> 16);
#pragma unroll
    for (int o = 32; o > 0; o >>= 1) tot += __shfl_xor(tot, o, 64);
    tot = __builtin_amdgcn_readfirstlane(tot);
    const bool fast = tot <= (uint32_t)CAP;
    const int nchunks = fast ? 1 : nblk;

    f32x16 Z0, Z1;
#pragma unroll
    for (int i = 0; i < 16; ++i) { Z0[i] = 0.f; Z1[i] = 0.f; }

    const uint32_t stg = LB + L_STG + w * STG_W;
    uint32_t hfa[4];
#pragma unroll
    for (int s = 0; s < 4; ++s) hfa[s] = stg + e * 128 + (((2 * s + h) ^ ((e >> 1) & 7)) << 4);
    const uint32_t lp0 = ((lane & 7) ^ ((lane >> 4) & 7)) << 4;
    const uint32_t lp1 = ((lane & 7) ^ ((4 + (lane >> 4)) & 7)) << 4;
    const uint32_t mbuf = stg + 8192;
    uint32_t mwa[4];
#pragma unroll
    for (int g = 0; g < 4; ++g) mwa[g] = mbuf + e * 64 + (((2 * g + h) ^ ((e >> 1) & 7)) << 3);
    uint32_t tra[2];
    {
        const int i16 = lane & 15, q = i16 >> 2, p = i16 & 3, bk = (lane >> 4) & 1;
#pragma unroll
        for (int u = 0; u < 2; ++u) {
            const int row = 8 * h + 4 * u + q;
            tra[u] = mbuf + row * 64 + (((4 * bk + p) ^ ((row >> 1) & 7)) << 3);
        }
    }

    int nb0 = 0, nb1 = 0;
    asm volatile("" :: "v"(W2[0]), "v"(W2[1]), "v"(W2[2]), "v"(W2[3]), "v"(W2[4]), "v"(W2[5]), "v"(W2[6]), "v"(W2[7]));
    asm volatile("" :: "v"(Wa[0]), "v"(Wa[1]), "v"(Wd[0]), "v"(Wd[1]), "v"(Wx[0]), "v"(Wx[1]), "v"(Wx[2]), "v"(Wx[3]));
    asm volatile("v_mov_b32 v248, 0\n\tv_mov_b32 v249, 0\n\tv_mov_b32 v250, 0\n\t"
                 "v_mov_b32 v252, 0\n\tv_mov_b32 v253, 0\n\tv_mov_b32 v254, 0" ::: ACLOB);

    for (int ci = 0; ci < nchunks; ++ci) {
        uint32_t segv = 0;
        if (!fast) {
            const uint32_t t0 = __shfl(T4[0], ci >> 2, 64), t1 = __shfl(T4[1], ci >> 2, 64), t2 = __shfl(T4[2], ci >> 2, 64), t3 = __shfl(T4[3], ci >> 2, 64);
            segv = (ci & 3) == 0 ? t0 : (ci & 3) == 1 ? t1 : (ci & 3) == 2 ? t2 : t3;
            segv = __builtin_amdgcn_readfirstlane(segv);
            if ((segv >> 16) == 0 && ci != 0) continue;
        }
        const uint32_t n = fast ? tot : (segv >> 16);
        if (ci != 0) __syncthreads();
        if (tid < 256) ldsst<uint32_t>(LB + L_HIST + tid * 4, 0u);
        __syncthreads();
        uint32_t rc[8], rp[8];
        const int g4 = lane >> 4, j16 = lane & 15;
        if (fast) {
#pragma unroll
            for (int p = 0; p < 8; ++p) {
                const int L = (p >> 2) * 32 + w * 4 + g4;
                const uint32_t tv = __shfl(T4[p & 3], L, 64);
                const uint32_t b = 4 * L + (p & 3);
                rp[p] = b * CHUNK + (tv & 0xFFFF) + j16;
                const bool ok = (uint32_t)j16 < (tv >> 16);
                rc[p] = ok ? R0[rp[p]] : 0xFFFFFFFFu;
            }
#pragma unroll
            for (int p = 0; p < 8; ++p)
                if (rc[p] != 0xFFFFFFFFu) __hip_atomic_fetch_add((__attribute__((address_space(3))) uint32_t*)(LB + L_HIST + (rc[p] >> 16) * 4), 1u, __ATOMIC_RELAXED, __HIP_MEMORY_SCOPE_WORKGROUP);
#pragma unroll 1
            for (int p = 0; p < 8; ++p) {
                const int L = (p >> 2) * 32 + w * 4 + g4;
                const uint32_t tsel = (p & 3) == 0 ? T4[0] : (p & 3) == 1 ? T4[1] : (p & 3) == 2 ? T4[2] : T4[3];
                const uint32_t tv = __shfl(tsel, L, 64);
                const uint32_t b = 4 * L + (p & 3);
                for (uint32_t i = 16 + j16; i < (tv >> 16); i += 16) {
                    uint32_t r = R0[b * CHUNK + (tv & 0xFFFF) + i];
                    __hip_atomic_fetch_add((__attribute__((address_space(3))) uint32_t*)(LB + L_HIST + (r >> 16) * 4), 1u, __ATOMIC_RELAXED, __HIP_MEMORY_SCOPE_WORKGROUP);
                }
            }
        } else {
            for (uint32_t i = tid; i < n; i += 512) {
                uint32_t r = R0[ci * CHUNK + (segv & 0xFFFF) + i];
                __hip_atomic_fetch_add((__attribute__((address_space(3))) uint32_t*)(LB + L_HIST + (r >> 16) * 4), 1u, __ATOMIC_RELAXED, __HIP_MEMORY_SCOPE_WORKGROUP);
            }
        }
        __syncthreads();
        if (w == 0) {
            u32x4 hv = ldsld<u32x4>(LB + L_HIST + lane * 16);
            uint32_t sum = hv[0] + hv[1] + hv[2] + hv[3], x = sum;
#pragma unroll
            for (int o = 1; o < 64; o <<= 1) { uint32_t y = __shfl_up(x, o, 64); if (lane >= o) x += y; }
            u32x4 st; st[0] = x - sum; st[1] = st[0] + hv[0]; st[2] = st[1] + hv[1]; st[3] = st[2] + hv[2];
            ldsst<u32x4>(LB + L_HIST + lane * 16, st);
            ldsst<u32x4>(LB + L_START + lane * 16, st);
        } else if (w == 1) {
            ldsst<uint16_t>(LB + L_SRC + (n + lane) * 2, (uint16_t)0);
            ldsst<uint32_t>(LB + L_W1 + (n + lane) * 4, 0u);
        }
        __syncthreads();
        auto place = [&](uint32_t r, uint32_t gp) {
            const uint32_t dl = r >> 16;
            const uint32_t p = __hip_atomic_fetch_add((__attribute__((address_space(3))) uint32_t*)(LB + L_HIST + dl * 4), 1u, __ATOMIC_RELAXED, __HIP_MEMORY_SCOPE_WORKGROUP);
            const uint32_t ei = R1[gp];
            ldsst<uint16_t>(LB + L_SRC + p * 2, (uint16_t)(r & 0xFFFF));
            ldsst<uint32_t>(LB + L_W1 + p * 4, ei | (dl << 24));
        };
        if (fast) {
#pragma unroll
            for (int p = 0; p < 8; ++p) if (rc[p] != 0xFFFFFFFFu) place(rc[p], rp[p]);
#pragma unroll 1
            for (int p = 0; p < 8; ++p) {
                const int L = (p >> 2) * 32 + w * 4 + g4;
                const uint32_t tsel = (p & 3) == 0 ? T4[0] : (p & 3) == 1 ? T4[1] : (p & 3) == 2 ? T4[2] : T4[3];
                const uint32_t tv = __shfl(tsel, L, 64);
                const uint32_t b = 4 * L + (p & 3);
                for (uint32_t i = 16 + j16; i < (tv >> 16); i += 16) { const uint32_t gp = b * CHUNK + (tv & 0xFFFF) + i; place(R0[gp], gp); }
            }
        } else {
            for (uint32_t i = tid; i < n; i += 512) { const uint32_t gp = ci * CHUNK + (segv & 0xFFFF) + i; place(R0[gp], gp); }
        }
        __syncthreads();
        if (ci == 0) {
            nb0 = (w * 49) >> 1; nb1 = ((w + 1) * 49) >> 1;
            if (fast) {
                uint32_t sv[4];
#pragma unroll
                for (int i = 0; i < 4; ++i) sv[i] = (lane + 64 * i < BINSZ) ? ldsld<uint32_t>(LB + L_START + (lane + 64 * i) * 4) : 0xFFFFFFFFu;
                int prev = 0; bool ok = true; int a0 = 0, a1 = 0;
#pragma unroll
                for (int k = 1; k <= 8; ++k) {
                    int nbk;
                    if (k == 8) nbk = BINSZ;
                    else {
                        const uint32_t target = (n * k) >> 3;
                        nbk = 0;
#pragma unroll
                        for (int i = 0; i < 4; ++i) nbk += __popcll(__ballot(sv[i] < target));
                    }
                    ok = ok && (nbk - prev <= 31);
                    if (k - 1 == w) { a0 = prev; a1 = nbk; }
                    prev = nbk;
                }
                if (ok) { nb0 = a0; nb1 = a1; }
            }
        }
        const int nn = nb1 - nb0;
        const uint32_t eb = __builtin_amdgcn_readfirstlane(ldsld<uint32_t>(LB + L_START + nb0 * 4));
        const uint32_t ee = __builtin_amdgcn_readfirstlane(ldsld<uint32_t>(LB + L_START + nb1 * 4));
        const int cntw = (int)(ee - eb);
        const int nt = (cntw + 31) >> 5;
        const int lo16 = 16 * ((int)ldsld<uint32_t>(LB + L_START + (nb0 + min(e, nn)) * 4) - (int)eb - 8 * h);

        uint32_t w1A = 0, w1B = 0;

        auto prefetch = [&](auto SET, int t, uint32_t slot, uint32_t& w1X) {
            constexpr int AE = 240 + decltype(SET)::value * 4, AP = 248 + decltype(SET)::value * 4;
            const uint32_t base = eb + 32 * t;
            const uint32_t sa = LB + L_SRC + (base + (lane >> 3)) * 2;
            const uint32_t s0 = ldsld<uint16_t>(sa), s1 = ldsld<uint16_t>(sa + 16), s2 = ldsld<uint16_t>(sa + 32), s3 = ldsld<uint16_t>(sa + 48);
            const uint32_t sm = ldsld<uint16_t>(LB + L_SRC + (base + e) * 2);
            const uint32_t w1 = ldsld<uint32_t>(LB + L_W1 + (base + e) * 4);
            dma16(slot, (s0 << 7) | lp0, hg);
            dma16(slot + 1024, (s1 << 7) | lp1, hg);
            dma16(slot + 2048, (s2 << 7) | lp0, hg);
            dma16(slot + 3072, (s3 << 7) | lp1, hg);
            const uint32_t eo = (w1 & 0xFFFFFFu) * 32 + 16 * h;
            asm volatile("global_load_dwordx4 v[%c2:%c3], %0, %1" :: "v"(eo), "s"(eag), "i"(AE), "i"(AE + 3) : "memory", ACLOB);
            asm volatile("s_mov_b32 exec_hi, 0\n\tglobal_load_dwordx3 v[%c2:%c3], %0, %1\n\ts_mov_b32 exec_hi, -1" :: "v"(sm << 4), "s"(pos4g), "i"(AP), "i"(AP + 2) : "memory", ACLOB);
            w1X = w1;
        };

        auto body = [&](auto SET, int t, uint32_t& w1X) {
            constexpr int AE = 240 + decltype(SET)::value * 4, AP = 248 + decltype(SET)::value * 4;
            constexpr uint32_t slotoff = decltype(SET)::value * 4096;
            if (t + 1 < nt) asm volatile("s_waitcnt vmcnt(6)" ::: "memory");
            else asm volatile("s_waitcnt vmcnt(0)" ::: "memory");
            const uint32_t dl = w1X >> 24;
            const uint32_t prow = LB + L_P + dl * PROW;
            const uint32_t pa = prow + 16 * h;
            f32x16 x0, x1;
#pragma unroll
            for (int g = 0; g < 4; ++g) {
                f32x4 a = ldsld<f32x4>(pa + 32 * g), b = ldsld<f32x4>(pa + 128 + 32 * g);
#pragma unroll
                for (int i = 0; i < 4; ++i) { x0[4 * g + i] = a[i]; x1[4 * g + i] = b[i]; }
            }
            const f32x4 pd = ldsld<f32x4>(prow + 256);
            bf16x8 hf[4];
#pragma unroll
            for (int s = 0; s < 4; ++s) hf[s] = ldsld<bf16x8>(hfa[s] + slotoff);
            float dx, dy, dz;
            asm volatile("v_sub_f32 %0, %3, v%c6\n\tv_sub_f32 %1, %4, v%c7\n\tv_sub_f32 %2, %5, v%c8"
                         : "=&v"(dx), "=&v"(dy), "=&v"(dz) : "v"(pd[0]), "v"(pd[1]), "v"(pd[2]), "i"(AP), "i"(AP + 1), "i"(AP + 2));
            const float d2 = dx * dx + dy * dy + dz * dz;
            u32x4 d2v; d2v[0] = pkbf(d2, 0.f); d2v[1] = 0; d2v[2] = 0; d2v[3] = 0;
            asm volatile("v_mfma_f32_32x32x16_bf16 %0, %1, v[%c2:%c3], %0" : "+v"(x0) : "v"(Wa[0]), "i"(AE), "i"(AE + 3));
            asm volatile("v_mfma_f32_32x32x16_bf16 %0, %1, v[%c2:%c3], %0" : "+v"(x1) : "v"(Wa[1]), "i"(AE), "i"(AE + 3));
            x0 = MFMA32(Wd[0], __builtin_bit_cast(bf16x8, d2v), x0); x1 = MFMA32(Wd[1], __builtin_bit_cast(bf16x8, d2v), x1);
#pragma unroll
            for (int s = 0; s < 4; ++s) { x0 = MFMA32(W2[s], hf[s], x0); x1 = MFMA32(W2[4 + s], hf[s], x1); }
            asm volatile("" :: "v"(hf[0]), "v"(hf[1]), "v"(hf[2]), "v"(hf[3]));
            if (t + 2 < nt) prefetch(SET, t + 2, stg + slotoff, w1X);
            const int lo = lo16 - t * 512;
            const uint32_t ta0 = (uint32_t)min(max(lo, 0), 128), ta1 = (uint32_t)min(max(lo - 256, 0), 128);
            u32x4 S0 = ldsld<u32x4>(LB + L_TAB + ta0), S1 = ldsld<u32x4>(LB + L_TAB + ta1);
#pragma unroll
            for (int i = 0; i < 4; ++i) {
                S0[i] ^= (uint32_t)__builtin_amdgcn_update_dpp(0, (int)S0[i], 0x130, 0xF, 0xF, false);
                S1[i] ^= (uint32_t)__builtin_amdgcn_update_dpp(0, (int)S1[i], 0x130, 0xF, 0xF, false);
            }
            const bf16x8 Sf0 = __builtin_bit_cast(bf16x8, S0), Sf1 = __builtin_bit_cast(bf16x8, S1);
            f32x16 cs;
#pragma unroll
            for (int i = 0; i < 16; ++i) cs[i] = 0.f;
#pragma unroll
            for (int fb = 0; fb < 2; ++fb) {
                const f32x16& x = fb == 0 ? x0 : x1;
                uint32_t m[8];
#pragma unroll
                for (int i = 0; i < 8; ++i) m[i] = pkbf(silu1(x[2 * i]), silu1(x[2 * i + 1]));
#pragma unroll
                for (int g = 0; g < 4; ++g) { u32x2 v; v[0] = m[2 * g]; v[1] = m[2 * g + 1]; ldsst<u32x2>(mwa[g], v); }
                u32x4 mb0, mb1;
#pragma unroll
                for (int i = 0; i < 4; ++i) { mb0[i] = m[i]; mb1[i] = m[4 + i]; }
                cs = MFMA32(Wx[2 * fb], __builtin_bit_cast(bf16x8, mb0), cs);
                cs = MFMA32(Wx[2 * fb + 1], __builtin_bit_cast(bf16x8, mb1), cs);
                const s16x4 a0 = trread(tra[0]), a1 = trread(tra[1]), a2 = trread(tra[0] + 1024), a3 = trread(tra[1] + 1024);
                const bf16x8 A0 = __builtin_shufflevector(a0, a1, 0, 1, 2, 3, 4, 5, 6, 7), A1 = __builtin_shufflevector(a2, a3, 0, 1, 2, 3, 4, 5, 6, 7);
                if (fb == 0) { Z0 = MFMA32(A0, Sf0, Z0); Z0 = MFMA32(A1, Sf1, Z0); }
                else { Z1 = MFMA32(A0, Sf0, Z1); Z1 = MFMA32(A1, Sf1, Z1); }
            }
            if (lane < 32 && t * 32 + lane < cntw) {
                const float cv = cs[0];
                __attribute__((address_space(3))) float* ap = (__attribute__((address_space(3))) float*)(prow + 272);
                __hip_atomic_fetch_add(ap, dx * cv, __ATOMIC_RELAXED, __HIP_MEMORY_SCOPE_WORKGROUP);
                __hip_atomic_fetch_add(ap + 1, dy * cv, __ATOMIC_RELAXED, __HIP_MEMORY_SCOPE_WORKGROUP);
                __hip_atomic_fetch_add(ap + 2, dz * cv, __ATOMIC_RELAXED, __HIP_MEMORY_SCOPE_WORKGROUP);
            }
        };

        if (nt > 0) prefetch(IC<0>{}, 0, stg, w1A);
        if (nt > 1) prefetch(IC<1>{}, 1, stg + 4096, w1B);
#pragma unroll 1
        for (int t = 0; t < nt; t += 2) {
            body(IC<0>{}, t, w1A);
            if (t + 1 < nt) body(IC<1>{}, t + 1, w1B);
        }
    }

    {
        const int nn = nb1 - nb0;
        const int nl = nb0 + e;
        const bool valid = e < nn && nl < nnodes;
        const int ng = min(node0 + nl, N - 1);
        bf16x8 hb[4];
#pragma unroll
        for (int s = 0; s < 4; ++s) hb[s] = *(const bf16x8*)(hg + (size_t)ng * 64 + 16 * s + 8 * h);
        u32x4 zb[4];
#pragma unroll
        for (int i = 0; i < 4; ++i) {
            zb[0][i] = pkbf(Z0[2 * i], Z0[2 * i + 1]); zb[1][i] = pkbf(Z0[8 + 2 * i], Z0[8 + 2 * i + 1]);
            zb[2][i] = pkbf(Z1[2 * i], Z1[2 * i + 1]); zb[3][i] = pkbf(Z1[8 + 2 * i], Z1[8 + 2 * i + 1]);
        }
#pragma unroll
        for (int ob = 0; ob < 2; ++ob) {
            f32x16 o;
#pragma unroll
            for (int i = 0; i < 16; ++i) o[i] = 0.f;
#pragma unroll
            for (int s = 0; s < 4; ++s) o = MFMA32(__builtin_bit_cast(bf16x8, WF[(24 + ob * 4 + s) * 64 + lane]), hb[s], o);
#pragma unroll
            for (int s = 0; s < 4; ++s) o = MFMA32(__builtin_bit_cast(bf16x8, WF[(32 + ob * 4 + s) * 64 + lane]), __builtin_bit_cast(bf16x8, zb[s]), o);
            if (valid) {
#pragma unroll
                for (int g = 0; g < 4; ++g) {
                    const size_t off = (size_t)ng * 64 + 32 * ob + 8 * g + 4 * h;
                    const u32x2 hv = *(const u32x2*)(hg + off);
                    u32x2 r;
                    r[0] = pkbf(bf2f(hv[0] & 0xFFFF) + silu1(o[4 * g]), bf2f(hv[0] >> 16) + silu1(o[4 * g + 1]));
                    r[1] = pkbf(bf2f(hv[1] & 0xFFFF) + silu1(o[4 * g + 2]), bf2f(hv[1] >> 16) + silu1(o[4 * g + 3]));
                    *(u32x2*)(out + off) = r;
                }
            }
        }
        if (valid && h == 0) {
            const uint32_t prow = LB + L_P + nl * PROW;
            const f32x4 pd = ldsld<f32x4>(prow + 256), ac = ldsld<f32x4>(prow + 272);
            uint16_t* po = out + (size_t)N * 64 + (size_t)ng * 3;
            po[0] = f2bf(pd[0] + ac[0]); po[1] = f2bf(pd[1] + ac[1]); po[2] = f2bf(pd[2] + ac[2]);
        }
    }
}

static DI float rbf(float f) { return bf2f(f2bf(f)); }
__global__ void fb_zero(float* p, int n) { int i = blockIdx.x * 256 + threadIdx.x; if (i < n) p[i] = 0.f; }
__global__ void fb_edge(const uint16_t* h, const uint16_t* pos, const uint16_t* ea, const int* src, const int* dst,
                        const uint16_t* we, const uint16_t* wx, float* mi, float* pa, int E) {
    int e = blockIdx.x * 4 + (threadIdx.x >> 6);
    int f = threadIdx.x & 63;
    if (e >= E) return;
    size_t s = src[e], d = dst[e];
    float df0 = bf2f(pos[d * 3]) - bf2f(pos[s * 3]), df1 = bf2f(pos[d * 3 + 1]) - bf2f(pos[s * 3 + 1]), df2 = bf2f(pos[d * 3 + 2]) - bf2f(pos[s * 3 + 2]);
    float d2 = rbf(df0 * df0 + df1 * df1 + df2 * df2);
    float acc = 0.f;
    for (int k = 0; k < 64; ++k) acc += bf2f(h[d * 64 + k]) * bf2f(we[k * 64 + f]);
    for (int k = 0; k < 64; ++k) acc += bf2f(h[s * 64 + k]) * bf2f(we[(64 + k) * 64 + f]);
    acc += d2 * bf2f(we[128 * 64 + f]);
    for (int k = 0; k < 16; ++k) acc += bf2f(ea[(size_t)e * 16 + k]) * bf2f(we[(129 + k) * 64 + f]);
    float m = rbf(silu1(acc));
    float cs = m * bf2f(wx[f]);
    for (int o = 32; o > 0; o >>= 1) cs += __shfl_xor(cs, o, 64);
    atomicAdd(&mi[d * 64 + f], m);
    if (f < 3) atomicAdd(&pa[d * 3 + f], (f == 0 ? df0 : f == 1 ? df1 : df2) * cs);
}
__global__ void fb_node(const uint16_t* h, const uint16_t* pos, const uint16_t* wh, const float* mi, const float* pa, uint16_t* out, int N) {
    int n = blockIdx.x * 4 + (threadIdx.x >> 6);
    int f = threadIdx.x & 63;
    if (n >= N) return;
    float acc = 0.f;
    for (int k = 0; k < 64; ++k) acc += bf2f(h[(size_t)n * 64 + k]) * bf2f(wh[k * 64 + f]);
    for (int k = 0; k < 64; ++k) acc += rbf(mi[(size_t)n * 64 + k]) * bf2f(wh[(64 + k) * 64 + f]);
    out[(size_t)n * 64 + f] = f2bf(bf2f(h[(size_t)n * 64 + f]) + silu1(acc));
    if (f < 3) out[(size_t)N * 64 + (size_t)n * 3 + f] = f2bf(bf2f(pos[(size_t)n * 3 + f]) + pa[(size_t)n * 3 + f]);
}

extern "C" void kernel_launch(void* const* d_in, const int* in_sizes, int n_in,
                              void* d_out, int out_size, void* d_ws, size_t ws_size,
                              hipStream_t stream) {
    const uint16_t* h = (const uint16_t*)d_in[0];
    const uint16_t* pos = (const uint16_t*)d_in[1];
    const uint16_t* ea = (const uint16_t*)d_in[2];
    const int* src = (const int*)d_in[3];
    const int* dst = (const int*)d_in[4];
    const uint16_t* we = (const uint16_t*)d_in[5];
    const uint16_t* wx = (const uint16_t*)d_in[6];
    const uint16_t* wh = (const uint16_t*)d_in[7];
    const int N = in_sizes[0] / 64, E = in_sizes[3];
    const int nbin = (N + BINSZ - 1) / BINSZ, nblk = (E + CHUNK - 1) / CHUNK;
    if (nbin > NBIN || nblk > NBLK || N < 1 || E < 1) {
        float* mi = (float*)d_ws; float* pa = mi + (size_t)N * 64;
        int nz = N * 67;
        fb_zero<<<(nz + 255) / 256, 256, 0, stream>>>(mi, nz);
        if (E > 0) fb_edge<<<(E + 3) / 4, 256, 0, stream>>>(h, pos, ea, src, dst, we, wx, mi, pa, E);
        fb_node<<<(N + 3) / 4, 256, 0, stream>>>(h, pos, wh, mi, pa, (uint16_t*)d_out, N);
        return;
    }
    char* ws = (char*)d_ws;
    uint32_t* T = (uint32_t*)ws;
    u32x4* WF = (u32x4*)(ws + 262144);
    f32x4* pos4 = (f32x4*)(ws + 327680);
    uint32_t* R0 = (uint32_t*)(ws + 327680 + 802816);
    uint32_t* R1 = R0 + NBLK * CHUNK;
    u32x4* EA2 = (u32x4*)(R1 + NBLK * CHUNK);
    const int lds1 = NKEY * 4 + 64 + CHUNK * 8 + (MOVE_EA ? CHUNK * 32 : 0);
    hipFuncSetAttribute((const void*)k1_partition, hipFuncAttributeMaxDynamicSharedMemorySize, lds1);
    hipFuncSetAttribute((const void*)k2_main, hipFuncAttributeMaxDynamicSharedMemorySize, L_TOTAL);
    k1_partition<<<nblk, 1024, lds1, stream>>>(src, dst, (const u32x4*)ea, pos, we, wx, wh, R0, R1, T, EA2, pos4, WF, N, E);
    k2_main<<<nbin, 512, L_TOTAL, stream>>>(h, MOVE_EA ? (const char*)EA2 : (const char*)ea, (const char*)pos4, R0, R1, T, WF, (uint16_t*)d_out, N, nblk);
}
```

```cpp
#include <hip/hip_runtime.h>
#include <hip/hip_bf16.h>
#include <stdint.h>

using u32x4 = __attribute__((ext_vector_type(4))) unsigned;
using u32x2 = __attribute__((ext_vector_type(2))) unsigned;
using f32x16 = __attribute__((ext_vector_type(16))) float;
using f32x4 = __attribute__((ext_vector_type(4))) float;
using f32x2 = __attribute__((ext_vector_type(2))) float;
using bf16x8 = __attribute__((ext_vector_type(8))) short;
using s16x4 = __attribute__((ext_vector_type(4))) short;
using bf16x2 = __attribute__((ext_vector_type(2))) __bf16;
#define DI __device__ __forceinline__
#define ACLOB "v240", "v241", "v242", "v243", "v244", "v245", "v246", "v247", "v248", "v249", "v250", "v251", "v252", "v253", "v254", "v255"
#define MFMA32(a, b, c) __builtin_amdgcn_mfma_f32_32x32x16_bf16((a), (b), (c), 0, 0, 0)
#define MFMA8(a, b, c) __builtin_amdgcn_mfma_f32_32x32x8bf16_1k((a), (b), (c), 0, 0, 0)
#define MFMA4(a, b, c) __builtin_amdgcn_mfma_f32_4x4x4bf16_1k((a), (b), (c), 0, 0, 0)
#define SB() __builtin_amdgcn_sched_barrier(0)
#define BS(x) __builtin_bit_cast(bf16x8, x)

constexpr int CHUNK = 3136;
constexpr int BINSZ = 196;
constexpr int NBIN = 256;
constexpr int NBLK = 256;
constexpr int CAP = 3392;
constexpr int TRASH = CAP + 64;
constexpr int LISTN = TRASH + 136;

constexpr int PROW = 272;
constexpr int L_P = 0;
constexpr int L_SRC = BINSZ * PROW;
constexpr int L_W1 = L_SRC + LISTN * 2;
constexpr int L_TAB = L_W1 + LISTN * 4;
constexpr int L_HIST = L_TAB + 160;
constexpr int L_FLAG = L_HIST + 320 * 4;
constexpr int L_STG = ((L_FLAG + 16 + 127) / 128) * 128;
constexpr int STG_W = 8192 + 2304;
constexpr int L_TOTAL = L_STG + 8 * STG_W;
static_assert(L_TOTAL <= 163840, "LDS");
static_assert(L_W1 % 16 == 0 && L_TAB % 16 == 0 && L_HIST % 16 == 0, "align");
constexpr float NLOG2E = -1.44269504089f;

DI float bf2f(uint32_t v) { return __uint_as_float(v << 16); }
DI uint32_t pkbf(float a, float b) { bf16x2 v; v[0] = (__bf16)a; v[1] = (__bf16)b; return __builtin_bit_cast(uint32_t, v); }
DI uint16_t f2bf(float a) { __bf16 v = (__bf16)a; return __builtin_bit_cast(uint16_t, v); }
DI float silu1(float x) { return x * __builtin_amdgcn_rcpf(1.f + __builtin_amdgcn_exp2f(x * NLOG2E)); }

DI uint32_t wave_scan(uint32_t x) {
    x += (uint32_t)__builtin_amdgcn_update_dpp(0, (int)x, 0x111, 0xF, 0xF, false);
    x += (uint32_t)__builtin_amdgcn_update_dpp(0, (int)x, 0x112, 0xF, 0xF, false);
    x += (uint32_t)__builtin_amdgcn_update_dpp(0, (int)x, 0x114, 0xF, 0xF, false);
    x += (uint32_t)__builtin_amdgcn_update_dpp(0, (int)x, 0x118, 0xF, 0xF, false);
    x += (uint32_t)__builtin_amdgcn_update_dpp(0, (int)x, 0x142, 0xA, 0xF, false);
    x += (uint32_t)__builtin_amdgcn_update_dpp(0, (int)x, 0x143, 0xC, 0xF, false);
    return x;
}

#define STAMPDECL
#define STAMP(K, i)
#define STAMPOUT(K)
#define HM(x) (x)

__global__ __launch_bounds__(1024) void k1_partition(const int* __restrict__ src, const int* __restrict__ dst,
                                                     const uint16_t* __restrict__ pos, const uint16_t* __restrict__ we, const uint16_t* __restrict__ wx,
                                                     const uint16_t* __restrict__ wh,
                                                     u32x2* __restrict__ R, uint32_t* __restrict__ T,
                                                     f32x4* __restrict__ pos4, u32x4* __restrict__ WF, int N, int E) {
    __shared__ __attribute__((aligned(16))) uint32_t hist[NBIN];
    __shared__ __attribute__((aligned(16))) u32x2 recS[CHUNK];
    const int tid = threadIdx.x, blk = blockIdx.x;
    const int e0 = blk * CHUNK;
    const int cnt = min(CHUNK, E - e0);
    STAMPDECL;
    STAMP(0, 0);
    if (tid < NBIN) hist[tid] = 0;
    int d[4], s[4];
#pragma unroll
    for (int r = 0; r < 4; ++r) { int i = r * 1024 + tid; d[r] = i < cnt ? dst[e0 + i] : 0; s[r] = i < cnt ? src[e0 + i] : 0; }
    STAMP(0, 1);
    __syncthreads();
    uint32_t bin[4], rank[4];
#pragma unroll
    for (int r = 0; r < 4; ++r) {
        int i = r * 1024 + tid;
        bin[r] = (uint32_t)d[r] / BINSZ;
        rank[r] = 0;
        if (i < cnt) rank[r] = atomicAdd(&hist[bin[r]], 1u);
    }
    __syncthreads();
    STAMP(0, 2);
    if (tid < 64) {
        u32x4 hv = *(const u32x4*)(hist + 4 * tid);
        const uint32_t sum = hv[0] + hv[1] + hv[2] + hv[3], x = wave_scan(sum);
        u32x4 st; st[0] = x - sum; st[1] = st[0] + hv[0]; st[2] = st[1] + hv[1]; st[3] = st[2] + hv[2];
        *(u32x4*)(hist + 4 * tid) = st;
#pragma unroll
        for (int i = 0; i < 4; ++i) T[(4 * tid + i) * NBLK + blk] = st[i] | (hv[i] << 16);
    }
    __syncthreads();
    STAMP(0, 3);
#pragma unroll
    for (int r = 0; r < 4; ++r) {
        int i = r * 1024 + tid;
        if (i < cnt) {
            const uint32_t dl = d[r] - bin[r] * BINSZ;
            u32x2 rec; rec[0] = (uint32_t)(e0 + i) | (dl << 24); rec[1] = (uint32_t)s[r] | (dl << 18);
            recS[hist[bin[r]] + rank[r]] = rec;
        }
    }
    __syncthreads();
    STAMP(0, 4);
#pragma unroll
    for (int r = 0; r < 2; ++r) {
        int i = r * 1024 + tid;
        if (2 * i < cnt) *(u32x4*)(R + e0 + 2 * i) = *(const u32x4*)(recS + 2 * i);
    }
    STAMP(0, 5);
    for (int n = blk * BINSZ + tid; tid < BINSZ && n < N; n += gridDim.x * BINSZ) {
        f32x4 p; p[0] = bf2f(pos[n * 3]); p[1] = bf2f(pos[n * 3 + 1]); p[2] = bf2f(pos[n * 3 + 2]); p[3] = 0.f; pos4[n] = p;
    }
    if (tid >= 960) {
        for (int f = blk; f < 40; f += gridDim.x) {
            const int l = tid - 960, r = l & 31, h = l >> 5;
            uint32_t o[4];
#pragma unroll
            for (int jj = 0; jj < 4; ++jj) {
                uint32_t e2[2];
#pragma unroll
                for (int u = 0; u < 2; ++u) {
                    const int j = 2 * jj + u;
                    const int kn = 8 * h + j, kp = 8 * (j >> 2) + 4 * h + (j & 3);
                    uint32_t val;
                    if (f < 8) { int fb = f >> 2, sx = f & 3; val = we[(64 + 16 * sx + kn) * 64 + 32 * fb + r]; }
                    else if (f < 10) { int fb = f - 8; val = we[(129 + kn) * 64 + 32 * fb + r]; }
                    else if (f < 12) { int fb = f - 10; val = (kn == 0) ? we[128 * 64 + 32 * fb + r] : 0; }
                    else if (f < 16) { int fb = (f - 12) >> 1, sx = (f - 12) & 1; val = wx[32 * fb + 16 * sx + kp]; }
                    else if (f < 24) { int fb = (f - 16) >> 2, sx = (f - 16) & 3; val = we[(16 * sx + kn) * 64 + 32 * fb + r]; }
                    else if (f < 32) { int ob = (f - 24) >> 2, sx = (f - 24) & 3; val = wh[(16 * sx + kn) * 64 + 32 * ob + r]; }
                    else { int ob = (f - 32) >> 2, fb = ((f - 32) >> 1) & 1, sx = (f - 32) & 1; val = wh[(64 + 32 * fb + 16 * sx + kp) * 64 + 32 * ob + r]; }
                    e2[u] = val;
                }
                o[jj] = e2[0] | (e2[1] << 16);
            }
            u32x4 ov; ov[0] = o[0]; ov[1] = o[1]; ov[2] = o[2]; ov[3] = o[3];
            WF[f * 64 + l] = ov;
        }
    }
    STAMP(0, 6);
    STAMPOUT(0);
}

DI void dma16(uint32_t ldsaddr, uint32_t voff, const void* base) {
    asm volatile("s_mov_b32 m0, %0\n\ts_nop 0\n\tglobal_load_lds_dwordx4 %1, %2" :: "s"(ldsaddr), "v"(voff), "s"(base) : "memory");
}
typedef __attribute__((address_space(3))) s16x4* lds_s16x4_p;
typedef __attribute__((address_space(3))) uint32_t* lds_u32_p;
DI s16x4 trread(uint32_t addr) { return __builtin_amdgcn_ds_read_tr16_b64_v4i16((lds_s16x4_p)(uintptr_t)addr); }
template <class T> DI T ldsld(uint32_t addr) { return *(__attribute__((address_space(3))) T*)(uintptr_t)addr; }
template <class T> DI void ldsst(uint32_t addr, T v) { *(__attribute__((address_space(3))) T*)(uintptr_t)addr = v; }
template <int V> struct IC { static constexpr int value = V; };

__global__ __launch_bounds__(512, 2) __attribute__((amdgpu_num_vgpr(120)))
void k2_main(const uint16_t* __restrict__ hg, const char* __restrict__ eag, const char* __restrict__ pos4g,
             const u32x2* __restrict__ R, const uint32_t* __restrict__ T,
             const u32x4* __restrict__ WF, uint16_t* __restrict__ out, int N, int nblk) {
    extern __shared__ __attribute__((aligned(16))) char ldsraw[];
    const uint32_t LB = (uint32_t)(uintptr_t)(__attribute__((address_space(3))) char*)ldsraw;
    const int tid = threadIdx.x, lane = tid & 63;
    const int w = __builtin_amdgcn_readfirstlane(tid >> 6);
    const int c = blockIdx.x;
    const int node0 = c * BINSZ;
    const int nnodes = min(BINSZ, N - node0);

    STAMPDECL;
    STAMP(1, 0);
    const uint32_t stg = LB + L_STG + w * STG_W;
#pragma unroll
    for (int i = 0; i < 3; ++i) dma16(stg + 4096 + i * 1024, (uint32_t)((3 * w + i) * 1024 + lane * 16), WF);
    u32x4 T4 = *(const u32x4*)(T + (size_t)c * NBLK + 4 * lane);
    if (tid < 324) ldsst<uint32_t>(LB + L_HIST + tid * 4, 0u);

    bf16x8 hb0[4];
    f32x4 pd0;
    {
        const int ng = min(node0 + 32 * w + (lane & 31), N - 1);
#pragma unroll
        for (int s = 0; s < 4; ++s) hb0[s] = *(const bf16x8*)(hg + (size_t)ng * 64 + 16 * s + 8 * (lane >> 5));
        pd0 = *(const f32x4*)(pos4g + (size_t)ng * 16);
        if (w == 7 && lane < 36) {
            const int k = lane >> 2, q = lane & 3;
            uint32_t v = (2 * q < k ? 0x3F80u : 0u) | (2 * q + 1 < k ? 0x3F800000u : 0u);
            ldsst<uint32_t>(LB + L_TAB + lane * 4, v);
        }
    }
    STAMP(1, 1);
    f32x16 Z0, Z1, Z2;
#pragma unroll
    for (int i = 0; i < 16; ++i) { Z0[i] = 0.f; Z1[i] = 0.f; Z2[i] = 0.f; }

    int nb0 = (w * 49) >> 1, nb1 = ((w + 1) * 49) >> 1;
    asm volatile("v_mov_b32 v248, 0\n\tv_mov_b32 v249, 0\n\tv_mov_b32 v250, 0\n\t"
                 "v_mov_b32 v252, 0\n\tv_mov_b32 v253, 0\n\tv_mov_b32 v254, 0" ::: ACLOB);

    int seg0 = 0, round = 0;
    bool first = true;
    for (;;) {
        int lq = __builtin_amdgcn_mbcnt_hi(~0u, __builtin_amdgcn_mbcnt_lo(~0u, 0u)); asm volatile("" : "+v"(lq));
        const int eq = lq & 31, hq = lq >> 5;
        if (!first) {
            T4 = *(const u32x4*)(T + (size_t)c * NBLK + 4 * lq);
            __syncthreads();
            if (w * 64 + lq < 324) ldsst<uint32_t>(LB + L_HIST + (w * 64 + lq) * 4, 0u);
#pragma unroll
            for (int i = 0; i < 3; ++i) dma16(stg + 4096 + i * 1024, (uint32_t)((3 * w + i) * 1024 + lq * 16), WF);
        }
        bool more = false;
        uint32_t sum = 0;
#pragma unroll
        for (int i = 0; i < 4; ++i) {
            const int sg = 4 * lq + i;
            int cn = (int)(T4[i] >> 16) - 32 * round;
            if (sg >= nblk) cn = 0;
            more = more || cn > 32;
            cn = min(max(cn, 0), 32);
            if (sg < seg0) cn = 0;
            T4[i] = cn > 0 ? (((T4[i] & 0xFFFF) + 32 * round) | ((uint32_t)cn << 16)) : 0u;
            sum += cn;
        }
        u32x4 ra[4], rb[4];
        int cn4[4];
        const int L = w * 8 + (lq >> 3);
#pragma unroll
        for (int p = 0; p < 4; ++p) {
            const uint32_t tv = __shfl(T4[p], L, 64);
            const u32x2* rp = R + (4 * L + p) * CHUNK + (tv & 0xFFFF) + 4 * (lq & 7);
            ra[p] = *(const u32x4*)rp; rb[p] = *(const u32x4*)(rp + 2);
            cn4[p] = (int)(tv >> 16) - 4 * (lq & 7);
        }
        const uint32_t xs = wave_scan(sum);
        uint32_t n = __builtin_amdgcn_readlane(xs, 63);
        int seg1 = NBLK;
        if (n > (uint32_t)CAP) {
            const int lcut = __popcll(__ballot(xs <= (uint32_t)CAP));
            n = __builtin_amdgcn_readlane(xs, lcut - 1);
            seg1 = 4 * lcut;
            if (L >= lcut) { cn4[0] = 0; cn4[1] = 0; cn4[2] = 0; cn4[3] = 0; }
        }
        const bool anymore = __any(more);
        const bool single = first && seg1 == NBLK && !anymore;
        T4 = u32x4{0u, 0u, 0u, 0u};
        asm volatile("s_waitcnt vmcnt(8) lgkmcnt(0)" ::: "memory");
        __builtin_amdgcn_s_barrier();
        STAMP(1, 2);
#define WFL(f) (LB + L_STG + ((f) / 3) * STG_W + 4096 + ((f) % 3) * 1024)
        if (first) {
            f32x16 p0, p1;
#pragma unroll
            for (int i = 0; i < 16; ++i) { p0[i] = 0.f; p1[i] = 0.f; }
#pragma unroll
            for (int s = 0; s < 4; ++s) {
                p0 = MFMA32(ldsld<bf16x8>(WFL(16 + s) + lq * 16), hb0[s], p0);
                p1 = MFMA32(ldsld<bf16x8>(WFL(20 + s) + lq * 16), hb0[s], p1);
            }
            const int nl = 32 * w + eq;
            if (nl < BINSZ) {
                const uint32_t pa = LB + L_P + nl * PROW + 16 * hq;
#pragma unroll
                for (int g = 0; g < 4; ++g) {
                    f32x4 a, b;
#pragma unroll
                    for (int i = 0; i < 4; ++i) { a[i] = p0[4 * g + i]; b[i] = p1[4 * g + i]; }
                    ldsst<f32x4>(pa + 32 * g, a); ldsst<f32x4>(pa + 128 + 32 * g, b);
                }
                if (hq == 0) ldsst<f32x4>(pa + 256, pd0);
            }
        }
#pragma unroll
        for (int s = 0; s < 4; ++s) hb0[s] = bf16x8{0, 0, 0, 0, 0, 0, 0, 0};
        pd0 = f32x4{0.f, 0.f, 0.f, 0.f};
        bf16x8 W2[8], Wa[2], Wx[4];
        s16x4 Wd[2];
#pragma unroll
        for (int i = 0; i < 8; ++i) W2[i] = ldsld<bf16x8>(WFL(i) + lq * 16);
#pragma unroll
        for (int i = 0; i < 2; ++i) { Wa[i] = ldsld<bf16x8>(WFL(8 + i) + lq * 16); Wd[i] = ldsld<s16x4>(WFL(10 + i) + lq * 16); }
#pragma unroll
        for (int i = 0; i < 4; ++i) Wx[i] = ldsld<bf16x8>(WFL(12 + i) + lq * 16);
        uint32_t ha[16], rk[16];
        const uint32_t dummy = 1024 + 4 * lq;
#pragma unroll
        for (int p = 0; p < 4; ++p) {
#pragma unroll
            for (int i = 0; i < 4; ++i) {
                const uint32_t w1v = i == 0 ? ra[p][1] : i == 1 ? ra[p][3] : i == 2 ? rb[p][1] : rb[p][3];
                ha[4 * p + i] = cn4[p] > i ? (w1v >> 16) : dummy;
                rk[4 * p + i] = __hip_atomic_fetch_add((lds_u32_p)(uintptr_t)(LB + L_HIST + ha[4 * p + i]), 1u, __ATOMIC_RELAXED, __HIP_MEMORY_SCOPE_WORKGROUP);
            }
        }
        STAMP(1, 3);
        __syncthreads();
        {
            const u32x4 hv = ldsld<u32x4>(LB + L_HIST + lq * 16);
            const uint32_t sm = hv[0] + hv[1] + hv[2] + hv[3], x = wave_scan(sm);
            u32x4 st; st[0] = x - sm; st[1] = st[0] + hv[0]; st[2] = st[1] + hv[1]; st[3] = st[2] + hv[2];
            ldsst<u32x4>(stg + lq * 16, st);
            ldsst<uint32_t>(stg + dummy, (uint32_t)TRASH);
            if (w == 1) { ldsst<uint16_t>(LB + L_SRC + (n + lq) * 2, (uint16_t)0); ldsst<uint32_t>(LB + L_W1 + (n + lq) * 4, 0u); }
            if (single) {
                const uint32_t t0 = (n * w) >> 3, t1 = (n * (w + 1)) >> 3;
                int a0 = 0, a1 = 0;
#pragma unroll
                for (int i = 0; i < 4; ++i) { a0 += __popcll(__ballot(st[i] < t0)); a1 += __popcll(__ballot(st[i] < t1)); }
                if (w == 7) a1 = BINSZ;
                a0 = min(a0, BINSZ); a1 = min(a1, BINSZ);
                if (a1 - a0 > 31) ldsst<uint32_t>(LB + L_FLAG, 1u);
                nb0 |= a0 << 16; nb1 |= a1 << 16;
            }
        }
        STAMP(1, 4);
#pragma unroll
        for (int k = 0; k < 16; ++k) rk[k] += ldsld<uint32_t>(stg + ha[k]);
#pragma unroll
        for (int p = 0; p < 4; ++p) {
#pragma unroll
            for (int i = 0; i < 4; ++i) {
                const uint32_t w0v = i == 0 ? ra[p][0] : i == 1 ? ra[p][2] : i == 2 ? rb[p][0] : rb[p][2];
                const uint32_t w1v = i == 0 ? ra[p][1] : i == 1 ? ra[p][3] : i == 2 ? rb[p][1] : rb[p][3];
                ldsst<uint16_t>(LB + L_SRC + rk[4 * p + i] * 2, (uint16_t)w1v);
                ldsst<uint32_t>(LB + L_W1 + rk[4 * p + i] * 4, w0v);
            }
        }
        STAMP(1, 5);
        asm volatile("" :: "v"(W2[0]), "v"(W2[1]), "v"(W2[2]), "v"(W2[3]), "v"(W2[4]), "v"(W2[5]), "v"(W2[6]), "v"(W2[7]));
        asm volatile("" :: "v"(Wa[0]), "v"(Wa[1]), "v"(Wd[0]), "v"(Wd[1]), "v"(Wx[0]), "v"(Wx[1]), "v"(Wx[2]), "v"(Wx[3]));
        __syncthreads();
        STAMP(1, 6);
        if (single) {
            const bool bad = __builtin_amdgcn_readfirstlane(ldsld<uint32_t>(LB + L_FLAG)) != 0;
            nb0 = bad ? (nb0 & 0xFFFF) : (nb0 >> 16); nb1 = bad ? (nb1 & 0xFFFF) : (nb1 >> 16);
        }
        first = false;
        const bool last = seg1 == NBLK && !anymore;
        if (seg1 == NBLK) { seg0 = 0; ++round; } else seg0 = seg1;
        const int nn = nb1 - nb0;
        const uint32_t lov = ldsld<uint32_t>(stg + (nb0 + min(eq, nn)) * 4);
        const uint32_t eb = __builtin_amdgcn_readfirstlane(lov);
        const uint32_t ee = __builtin_amdgcn_readlane(lov, 31);
        const int cntw = (int)(ee - eb);
        const int nt = (cntw + 31) >> 5;
        int lo = 16 * ((int)lov - (int)eb - 8 * hq);
        STAMP(1, 7);
        if (nt == 0) { if (last) break; continue; }

        uint32_t hfa[4];
#pragma unroll
        for (int s = 0; s < 4; ++s) hfa[s] = stg + eq * 128 + (((2 * s + hq) ^ ((eq >> 1) & 7)) << 4);
        const uint32_t lp0 = ((lq & 7) ^ ((lq >> 4) & 7)) << 4;
        const uint32_t lp1 = ((lq & 7) ^ ((4 + (lq >> 4)) & 7)) << 4;
        const uint32_t mbuf = stg + 8192;
        const uint32_t mwa = mbuf + eq * 72 + 8 * hq;
        const uint32_t tra = mbuf + (8 * hq + ((lq & 15) >> 2)) * 72 + ((lq >> 4) & 1) * 32 + (lq & 3) * 8;
        const uint32_t vwa = mbuf + lq * 8;
        const uint32_t vra = mbuf + (8 * hq + ((lq & 15) >> 2)) * 8;
        const uint32_t lsa = LB + L_SRC + (lq >> 3) * 2;
        const uint32_t lse = LB + L_SRC + eq * 2;
        const uint32_t lw1 = LB + L_W1 + eq * 4;

        uint32_t w1A = 0, w1B = 0;
        uint32_t ix0, ix1, ix2, ix3, ixm;
        auto pf_idx = [&](int t, uint32_t& w1X) {
            const uint32_t li = t < nt ? eb + 32 * t : n;
            const uint32_t sa = lsa + 2 * li;
            ix0 = ldsld<uint16_t>(sa); ix1 = ldsld<uint16_t>(sa + 16); ix2 = ldsld<uint16_t>(sa + 32); ix3 = ldsld<uint16_t>(sa + 48);
            ixm = ldsld<uint16_t>(lse + 2 * li);
            w1X = ldsld<uint32_t>(lw1 + 4 * li);
        };
        auto pf_dma = [&](auto SET, int q, uint32_t ix) { dma16(stg + decltype(SET)::value * 4096 + q * 1024, (HM(ix) << 7) | ((q & 1) ? lp1 : lp0), hg); };
        auto pf_ea = [&](auto SET, uint32_t w1) {
            constexpr int AE = 240 + decltype(SET)::value * 4;
            const uint32_t eo = ((HM(w1) << 5) & 0x1FFFFFE0u) | (16 * hq);
            const char* bp = eag;
            asm volatile("global_load_dwordx4 v[%c2:%c3], %0, %1" :: "v"(eo), "s"(bp), "i"(AE), "i"(AE + 3) : "memory", ACLOB);
        };
        auto pf_pos = [&](auto SET) {
            constexpr int AP = 248 + decltype(SET)::value * 4;
            const char* bp = pos4g; const uint32_t po = HM(ixm) << 4;
            asm volatile("s_mov_b32 exec_hi, 0\n\tglobal_load_dwordx3 v[%c2:%c3], %0, %1\n\ts_mov_b32 exec_hi, -1" :: "v"(po), "s"(bp), "i"(AP), "i"(AP + 2) : "memory", ACLOB);
        };
        auto prefetch = [&](auto SET, int t, uint32_t& w1X) {
            pf_idx(t, w1X);
            pf_dma(SET, 0, ix0); pf_dma(SET, 1, ix1); pf_dma(SET, 2, ix2); pf_dma(SET, 3, ix3);
            pf_ea(SET, w1X); pf_pos(SET);
        };

        f32x16 x0, x1;
        bf16x8 hf[4];
        s16x4 d2v;
        float dxc, dyc, dzc;
        float dxp = 0.f, dyp = 0.f, dzp = 0.f;
        uint32_t pac;
        uint32_t m[8];
#pragma unroll
        for (int i = 0; i < 8; ++i) m[i] = 0u;
        f32x4 cs = {0.f, 0.f, 0.f, 0.f};
        u32x4 S0 = {0u, 0u, 0u, 0u}, S1 = {0u, 0u, 0u, 0u};
        bf16x8 A0, A1;
#pragma unroll
        for (int i = 0; i < 8; ++i) { A0[i] = 0; A1[i] = 0; }
        f32x4 pd;

        auto ldP = [&](f32x16& x, uint32_t a) {
#pragma unroll
            for (int g = 0; g < 4; ++g) {
                const f32x4 v = ldsld<f32x4>(a + 32 * g);
#pragma unroll
                for (int i = 0; i < 4; ++i) x[4 * g + i] = v[i];
            }
        };
        auto load_tile = [&](auto SET, uint32_t w1X) {
            const uint32_t prow = LB + L_P + (w1X >> 24) * PROW;
            pac = prow + 16 * hq;
            pd = ldsld<f32x4>(prow + 256);
            ldP(x0, pac);
#pragma unroll
            for (int s = 0; s < 4; ++s) hf[s] = ldsld<bf16x8>(hfa[s] + decltype(SET)::value * 4096);
        };
        auto mkdiff = [&](auto SET) {
            constexpr int AP = 248 + decltype(SET)::value * 4;
            const float p0 = pd[0], p1 = pd[1], p2 = pd[2]; float q0, q1, q2;
            asm volatile("v_sub_f32 %0, %3, v%c6\n\tv_sub_f32 %1, %4, v%c7\n\tv_sub_f32 %2, %5, v%c8"
                         : "=&v"(q0), "=&v"(q1), "=&v"(q2) : "v"(p0), "v"(p1), "v"(p2), "i"(AP), "i"(AP + 1), "i"(AP + 2));
            dxc = q0; dyc = q1; dzc = q2;
            const float d2 = dxc * dxc + dyc * dyc + dzc * dzc;
            u32x2 dv; dv[0] = pkbf(d2, d2); dv[1] = dv[0];
            d2v = __builtin_bit_cast(s16x4, dv);
        };
        auto posupd = [&]() {
            const uint32_t c0 = __float_as_uint(cs[0]);
            const auto sw = __builtin_amdgcn_permlane32_swap(c0, c0, false, false);
            const float cv = __uint_as_float(sw[0]) + __uint_as_float(sw[1]);
            u32x2 vv; vv[0] = pkbf(dxp * cv, dyp * cv); vv[1] = pkbf(dzp * cv, dzp);
            ldsst<u32x2>(vwa, vv);
            const s16x4 a0 = trread(vra), a1 = trread(vra + 32), a2 = trread(vra + 128), a3 = trread(vra + 160);
            A0 = __builtin_shufflevector(a0, a1, 0, 1, 2, 3, 4, 5, 6, 7); A1 = __builtin_shufflevector(a2, a3, 0, 1, 2, 3, 4, 5, 6, 7);
            cs = f32x4{0.f, 0.f, 0.f, 0.f};
            dxp = dxc; dyp = dyc; dzp = dzc;
        };
        auto ea_mfma = [&](auto SET, f32x16& x, const bf16x8& wa) {
            constexpr int AE = 240 + decltype(SET)::value * 4;
            asm volatile("v_mfma_f32_32x32x16_bf16 %0, %1, v[%c2:%c3], %0" : "+v"(x) : "v"(wa), "i"(AE), "i"(AE + 3));
        };
        auto S_read = [&]() {
            const uint32_t ta0 = (uint32_t)min(max(lo, 0), 128), ta1 = (uint32_t)min(max(lo - 256, 0), 128);
            lo -= 512;
            S0 = ldsld<u32x4>(LB + L_TAB + ta0); S1 = ldsld<u32x4>(LB + L_TAB + ta1);
        };
        auto S_xor = [&]() {
#pragma unroll
            for (int i = 0; i < 4; ++i) {
                S0[i] ^= (uint32_t)__builtin_amdgcn_update_dpp(0, (int)S0[i], 0x130, 0xF, 0xF, false);
                S1[i] ^= (uint32_t)__builtin_amdgcn_update_dpp(0, (int)S1[i], 0x130, 0xF, 0xF, false);
            }
            asm volatile("" : "+v"(S0), "+v"(S1));
        };
        auto mwrite_tr = [&]() {
#pragma unroll
            for (int g = 0; g < 4; ++g) { u32x2 v; v[0] = m[2 * g]; v[1] = m[2 * g + 1]; ldsst<u32x2>(mwa + 16 * g, v); }
            const s16x4 a0 = trread(tra), a1 = trread(tra + 288), a2 = trread(tra + 1152), a3 = trread(tra + 1440);
            A0 = __builtin_shufflevector(a0, a1, 0, 1, 2, 3, 4, 5, 6, 7); A1 = __builtin_shufflevector(a2, a3, 0, 1, 2, 3, 4, 5, 6, 7);
        };
        auto csq = [&](int fb, int q) {
            const bf16x8& wv = Wx[2 * fb + (q >> 1)];
            const s16x4 a = (q & 1) ? __builtin_shufflevector(wv, wv, 4, 5, 6, 7) : __builtin_shufflevector(wv, wv, 0, 1, 2, 3);
            u32x2 b; b[0] = m[2 * q]; b[1] = m[2 * q + 1];
            cs = MFMA4(a, __builtin_bit_cast(s16x4, b), cs);
        };
#define SILU4(x, b, mo, SLOT1, SLOT2, SLOT3) do { \
            float t0 = x[4 * b] * NLOG2E, t1 = x[4 * b + 1] * NLOG2E, t2 = x[4 * b + 2] * NLOG2E, t3 = x[4 * b + 3] * NLOG2E; SB(); \
            SLOT1; SB(); \
            t0 = __builtin_amdgcn_exp2f(t0); t1 = __builtin_amdgcn_exp2f(t1); t2 = __builtin_amdgcn_exp2f(t2); t3 = __builtin_amdgcn_exp2f(t3); SB(); \
            SLOT2; SB(); \
            t0 += 1.f; t1 += 1.f; t2 += 1.f; t3 += 1.f; SB(); \
            t0 = __builtin_amdgcn_rcpf(t0); t1 = __builtin_amdgcn_rcpf(t1); t2 = __builtin_amdgcn_rcpf(t2); t3 = __builtin_amdgcn_rcpf(t3); SB(); \
            SLOT3; SB(); \
            t0 *= x[4 * b]; t1 *= x[4 * b + 1]; t2 *= x[4 * b + 2]; t3 *= x[4 * b + 3]; SB(); \
            mo[2 * b] = pkbf(t0, t1); mo[2 * b + 1] = pkbf(t2, t3); asm volatile("" : "+v"(mo[2 * b]), "+v"(mo[2 * b + 1])); SB(); } while (0)

        auto body = [&](auto SET, auto NXT, int t, uint32_t& w1X, uint32_t& w1N) {
            uint32_t mn[8];
            ldP(x1, pac + 128);
            pf_idx(t + 2, w1X);
            SB();
            SILU4(x0, 0, mn, (csq(1, 0), csq(1, 1)), (csq(1, 2), csq(1, 3)), Z1 = MFMA32(A0, BS(S0), Z1));
            SILU4(x0, 1, mn, Z1 = MFMA32(A1, BS(S1), Z1), ea_mfma(SET, x1, Wa[1]), (x1 = MFMA8(Wd[1], d2v, x1), posupd()));
            SILU4(x0, 2, mn, (x1 = MFMA32(W2[4], hf[0], x1), pf_dma(SET, 0, ix0)), (x1 = MFMA32(W2[5], hf[1], x1), pf_dma(SET, 1, ix1)), (x1 = MFMA32(W2[6], hf[2], x1), pf_dma(SET, 2, ix2)));
            SILU4(x0, 3, mn, (x1 = MFMA32(W2[7], hf[3], x1), pf_dma(SET, 3, ix3)), (Z2 = MFMA32(A0, BS(S0), Z2), pf_ea(SET, w1X)), (Z2 = MFMA32(A1, BS(S1), Z2), pf_pos(SET)));
#pragma unroll
            for (int i = 0; i < 8; ++i) m[i] = mn[i];
            mwrite_tr();
            S_read();
            asm volatile("s_waitcnt vmcnt(6)" ::: "memory");
            load_tile(NXT, w1N);
            SB();
            SILU4(x1, 0, mn, (csq(0, 0), csq(0, 1)), (csq(0, 2), csq(0, 3), S_xor()), Z0 = MFMA32(A0, BS(S0), Z0));
            SILU4(x1, 1, mn, (Z0 = MFMA32(A1, BS(S1), Z0), mkdiff(NXT)), ea_mfma(NXT, x0, Wa[0]), x0 = MFMA8(Wd[0], d2v, x0));
            SILU4(x1, 2, mn, x0 = MFMA32(W2[0], hf[0], x0), x0 = MFMA32(W2[1], hf[1], x0), x0 = MFMA32(W2[2], hf[2], x0));
            SILU4(x1, 3, mn, x0 = MFMA32(W2[3], hf[3], x0), (void)0, (void)0);
#pragma unroll
            for (int i = 0; i < 8; ++i) m[i] = mn[i];
            mwrite_tr();
            SB();
        };

        prefetch(IC<0>{}, 0, w1A);
        prefetch(IC<1>{}, 1, w1B);
        asm volatile("s_waitcnt vmcnt(6)" ::: "memory");
        load_tile(IC<0>{}, w1A);
        mkdiff(IC<0>{});
        ea_mfma(IC<0>{}, x0, Wa[0]);
        x0 = MFMA8(Wd[0], d2v, x0);
#pragma unroll
        for (int s = 0; s < 4; ++s) x0 = MFMA32(W2[s], hf[s], x0);
#pragma unroll 1
        for (int t = 0; t < nt; t += 2) {
            body(IC<0>{}, IC<1>{}, t, w1A, w1B);
            if (t + 1 < nt) body(IC<1>{}, IC<0>{}, t + 1, w1B, w1A);
        }
        asm volatile("s_waitcnt vmcnt(0)" ::: "memory");
        csq(1, 0); csq(1, 1); csq(1, 2); csq(1, 3);
        Z1 = MFMA32(A0, BS(S0), Z1);
        Z1 = MFMA32(A1, BS(S1), Z1);
        posupd();
        Z2 = MFMA32(A0, BS(S0), Z2);
        Z2 = MFMA32(A1, BS(S1), Z2);
        if (last) break;
    }

    STAMP(1, 8);
    {
        int lz = __builtin_amdgcn_mbcnt_hi(~0u, __builtin_amdgcn_mbcnt_lo(~0u, 0u)); asm volatile("" : "+v"(lz));
        const int ez = lz & 31, hz = lz >> 5;
        const int nn = nb1 - nb0;
        const int nl = nb0 + ez;
        const bool valid = ez < nn && nl < nnodes;
        const int ng = min(node0 + nl, N - 1);
        bf16x8 hb[4];
#pragma unroll
        for (int s = 0; s < 4; ++s) hb[s] = *(const bf16x8*)(hg + (size_t)ng * 64 + 16 * s + 8 * hz);
        u32x2 hv[8];
#pragma unroll
        for (int i = 0; i < 8; ++i) hv[i] = *(const u32x2*)(hg + (size_t)ng * 64 + 8 * i + 4 * hz);
        bf16x8 wf[16];
#pragma unroll
        for (int i = 0; i < 16; ++i) wf[i] = BS(WF[(24 + i) * 64 + lz]);
        u32x4 zb[4];
#pragma unroll
        for (int i = 0; i < 4; ++i) {
            zb[0][i] = pkbf(Z0[2 * i], Z0[2 * i + 1]); zb[1][i] = pkbf(Z0[8 + 2 * i], Z0[8 + 2 * i + 1]);
            zb[2][i] = pkbf(Z1[2 * i], Z1[2 * i + 1]); zb[3][i] = pkbf(Z1[8 + 2 * i], Z1[8 + 2 * i + 1]);
        }
#pragma unroll
        for (int ob = 0; ob < 2; ++ob) {
            f32x16 o;
#pragma unroll
            for (int i = 0; i < 16; ++i) o[i] = 0.f;
#pragma unroll
            for (int s = 0; s < 4; ++s) o = MFMA32(wf[ob * 4 + s], hb[s], o);
#pragma unroll
            for (int s = 0; s < 4; ++s) o = MFMA32(wf[8 + ob * 4 + s], BS(zb[s]), o);
            if (valid) {
#pragma unroll
                for (int g = 0; g < 4; ++g) {
                    const size_t off = (size_t)ng * 64 + 32 * ob + 8 * g + 4 * hz;
                    const u32x2 v = hv[4 * ob + g];
                    u32x2 r;
                    r[0] = pkbf(bf2f(v[0] & 0xFFFF) + silu1(o[4 * g]), bf2f(v[0] >> 16) + silu1(o[4 * g + 1]));
                    r[1] = pkbf(bf2f(v[1] & 0xFFFF) + silu1(o[4 * g + 2]), bf2f(v[1] >> 16) + silu1(o[4 * g + 3]));
                    *(u32x2*)(out + off) = r;
                }
            }
        }
        if (valid && hz == 0) {
            const uint32_t prow = LB + L_P + nl * PROW;
            const f32x4 pd = ldsld<f32x4>(prow + 256);
            uint16_t* po = out + (size_t)N * 64 + (size_t)ng * 3;
            po[0] = f2bf(pd[0] + Z2[0]); po[1] = f2bf(pd[1] + Z2[1]); po[2] = f2bf(pd[2] + Z2[2]);
        }
    }
    STAMP(1, 9);
    STAMPOUT(1);
}

static DI float rbf(float f) { return bf2f(f2bf(f)); }
__global__ void fb_zero(float* p, int n) { int i = blockIdx.x * 256 + threadIdx.x; if (i < n) p[i] = 0.f; }
__global__ void fb_edge(const uint16_t* h, const uint16_t* pos, const uint16_t* ea, const int* src, const int* dst,
                        const uint16_t* we, const uint16_t* wx, float* mi, float* pa, int E) {
    int e = blockIdx.x * 4 + (threadIdx.x >> 6);
    int f = threadIdx.x & 63;
    if (e >= E) return;
    size_t s = src[e], d = dst[e];
    float df0 = bf2f(pos[d * 3]) - bf2f(pos[s * 3]), df1 = bf2f(pos[d * 3 + 1]) - bf2f(pos[s * 3 + 1]), df2 = bf2f(pos[d * 3 + 2]) - bf2f(pos[s * 3 + 2]);
    float d2 = rbf(df0 * df0 + df1 * df1 + df2 * df2);
    float acc = 0.f;
    for (int k = 0; k < 64; ++k) acc += bf2f(h[d * 64 + k]) * bf2f(we[k * 64 + f]);
    for (int k = 0; k < 64; ++k) acc += bf2f(h[s * 64 + k]) * bf2f(we[(64 + k) * 64 + f]);
    acc += d2 * bf2f(we[128 * 64 + f]);
    for (int k = 0; k < 16; ++k) acc += bf2f(ea[(size_t)e * 16 + k]) * bf2f(we[(129 + k) * 64 + f]);
    float m = rbf(silu1(acc));
    float cs = m * bf2f(wx[f]);
    for (int o = 32; o > 0; o >>= 1) cs += __shfl_xor(cs, o, 64);
    atomicAdd(&mi[d * 64 + f], m);
    if (f < 3) atomicAdd(&pa[d * 3 + f], (f == 0 ? df0 : f == 1 ? df1 : df2) * cs);
}
__global__ void fb_node(const uint16_t* h, const uint16_t* pos, const uint16_t* wh, const float* mi, const float* pa, uint16_t* out, int N) {
    int n = blockIdx.x * 4 + (threadIdx.x >> 6);
    int f = threadIdx.x & 63;
    if (n >= N) return;
    float acc = 0.f;
    for (int k = 0; k < 64; ++k) acc += bf2f(h[(size_t)n * 64 + k]) * bf2f(wh[k * 64 + f]);
    for (int k = 0; k < 64; ++k) acc += rbf(mi[(size_t)n * 64 + k]) * bf2f(wh[(64 + k) * 64 + f]);
    out[(size_t)n * 64 + f] = f2bf(bf2f(h[(size_t)n * 64 + f]) + silu1(acc));
    if (f < 3) out[(size_t)N * 64 + (size_t)n * 3 + f] = f2bf(bf2f(pos[(size_t)n * 3 + f]) + pa[(size_t)n * 3 + f]);
}

extern "C" void kernel_launch(void* const* d_in, const int* in_sizes, int n_in,
                              void* d_out, int out_size, void* d_ws, size_t ws_size,
                              hipStream_t stream) {
    const uint16_t* h = (const uint16_t*)d_in[0];
    const uint16_t* pos = (const uint16_t*)d_in[1];
    const uint16_t* ea = (const uint16_t*)d_in[2];
    const int* src = (const int*)d_in[3];
    const int* dst = (const int*)d_in[4];
    const uint16_t* we = (const uint16_t*)d_in[5];
    const uint16_t* wx = (const uint16_t*)d_in[6];
    const uint16_t* wh = (const uint16_t*)d_in[7];
    const int N = in_sizes[0] / 64, E = in_sizes[3];
    const int nbin = (N + BINSZ - 1) / BINSZ, nblk = (E + CHUNK - 1) / CHUNK;
    if (nbin > NBIN || nblk > NBLK || N < 1 || E < 1) {
        float* mi = (float*)d_ws; float* pa = mi + (size_t)N * 64;
        int nz = N * 67;
        fb_zero<<<(nz + 255) / 256, 256, 0, stream>>>(mi, nz);
        if (E > 0) fb_edge<<<(E + 3) / 4, 256, 0, stream>>>(h, pos, ea, src, dst, we, wx, mi, pa, E);
        fb_node<<<(N + 3) / 4, 256, 0, stream>>>(h, pos, wh, mi, pa, (uint16_t*)d_out, N);
        return;
    }
    char* ws = (char*)d_ws;
    uint32_t* T = (uint32_t*)ws;
    u32x4* WF = (u32x4*)(ws + 262144);
    f32x4* pos4 = (f32x4*)(ws + 327680);
    u32x2* R = (u32x2*)(ws + 327680 + 802816);
    (void)hipFuncSetAttribute((const void*)k2_main, hipFuncAttributeMaxDynamicSharedMemorySize, L_TOTAL);
    k1_partition<<<nblk, 1024, 0, stream>>>(src, dst, pos, we, wx, wh, R, T, pos4, WF, N, E);
    k2_main<<<nbin, 512, L_TOTAL, stream>>>(h, (const char*)ea, (const char*)pos4, R, T, WF, (uint16_t*)d_out, N, nblk);
}
```

```cpp
#include <hip/hip_runtime.h>
#include <hip/hip_bf16.h>
#include <stdint.h>

using u32x4 = __attribute__((ext_vector_type(4))) unsigned;
using u32x2 = __attribute__((ext_vector_type(2))) unsigned;
using f32x16 = __attribute__((ext_vector_type(16))) float;
using f32x4 = __attribute__((ext_vector_type(4))) float;
using f32x2 = __attribute__((ext_vector_type(2))) float;
using bf16x8 = __attribute__((ext_vector_type(8))) short;
using s16x4 = __attribute__((ext_vector_type(4))) short;
using bf16x2 = __attribute__((ext_vector_type(2))) __bf16;
#define DI __device__ __forceinline__
#define ACLOB "v240", "v241", "v242", "v243", "v244", "v245", "v246", "v247", "v248", "v249", "v250", "v251", "v252", "v253", "v254", "v255"
#define MFMA32(a, b, c) __builtin_amdgcn_mfma_f32_32x32x16_bf16((a), (b), (c), 0, 0, 0)
#define MFMA8(a, b, c) __builtin_amdgcn_mfma_f32_32x32x8bf16_1k((a), (b), (c), 0, 0, 0)
#define MFMA4(a, b, c) __builtin_amdgcn_mfma_f32_4x4x4bf16_1k((a), (b), (c), 0, 0, 0)
#define SB() __builtin_amdgcn_sched_barrier(0)
#define BS(x) __builtin_bit_cast(bf16x8, x)

constexpr int CHUNK = 3136;
constexpr int BINSZ = 196;
constexpr int NBIN = 256;
constexpr int NBLK = 256;
constexpr int CAP = 3392;
constexpr int TRASH = CAP + 64;
constexpr int LISTN = TRASH + 136;

constexpr int PROW = 272;
constexpr int L_P = 0;
constexpr int L_SRC = BINSZ * PROW;
constexpr int L_W1 = L_SRC + LISTN * 2;
constexpr int L_TAB = L_W1 + LISTN * 4;
constexpr int L_HIST = L_TAB + 160;
constexpr int L_FLAG = L_HIST + 320 * 4;
constexpr int L_STG = ((L_FLAG + 16 + 127) / 128) * 128;
constexpr int STG_W = 8192 + 2304;
constexpr int L_TOTAL = L_STG + 8 * STG_W;
static_assert(L_TOTAL <= 163840, "LDS");
static_assert(L_W1 % 16 == 0 && L_TAB % 16 == 0 && L_HIST % 16 == 0, "align");
constexpr float NLOG2E = -1.44269504089f;

DI float bf2f(uint32_t v) { return __uint_as_float(v << 16); }
DI uint32_t pkbf(float a, float b) { bf16x2 v; v[0] = (__bf16)a; v[1] = (__bf16)b; return __builtin_bit_cast(uint32_t, v); }
DI uint16_t f2bf(float a) { __bf16 v = (__bf16)a; return __builtin_bit_cast(uint16_t, v); }
DI float silu1(float x) { return x * __builtin_amdgcn_rcpf(1.f + __builtin_amdgcn_exp2f(x * NLOG2E)); }

DI uint32_t wave_scan(uint32_t x) {
    x += (uint32_t)__builtin_amdgcn_update_dpp(0, (int)x, 0x111, 0xF, 0xF, false);
    x += (uint32_t)__builtin_amdgcn_update_dpp(0, (int)x, 0x112, 0xF, 0xF, false);
    x += (uint32_t)__builtin_amdgcn_update_dpp(0, (int)x, 0x114, 0xF, 0xF, false);
    x += (uint32_t)__builtin_amdgcn_update_dpp(0, (int)x, 0x118, 0xF, 0xF, false);
    x += (uint32_t)__builtin_amdgcn_update_dpp(0, (int)x, 0x142, 0xA, 0xF, false);
    x += (uint32_t)__builtin_amdgcn_update_dpp(0, (int)x, 0x143, 0xC, 0xF, false);
    return x;
}

#define STAMPDECL
#define STAMP(K, i)
#define STAMPOUT(K)
#define EAPOL ""
#define HM(x) (x)
#define HE(x) (x)

DI const uint16_t* wf_src(int f, int l, int j, const uint16_t* we, const uint16_t* wx, const uint16_t* wh, bool& ok) {
    const int r = l & 31, h = l >> 5;
    const int kn = 8 * h + j, kp = 8 * (j >> 2) + 4 * h + (j & 3);
    ok = true;
    if (f < 8) return we + (64 + 16 * (f & 3) + kn) * 64 + 32 * (f >> 2) + r;
    if (f < 10) return we + (129 + kn) * 64 + 32 * (f - 8) + r;
    if (f < 12) { ok = kn == 0; return we + 128 * 64 + 32 * (f - 10) + r; }
    if (f < 16) return wx + 32 * ((f - 12) >> 1) + 16 * ((f - 12) & 1) + kp;
    if (f < 24) return we + (16 * ((f - 16) & 3) + kn) * 64 + 32 * ((f - 16) >> 2) + r;
    if (f < 32) return wh + (16 * ((f - 24) & 3) + kn) * 64 + 32 * ((f - 24) >> 2) + r;
    return wh + (64 + 32 * (((f - 32) >> 1) & 1) + 16 * ((f - 32) & 1) + kp) * 64 + 32 * ((f - 32) >> 2) + r;
}

__global__ __launch_bounds__(1024) void k1_partition(const int* __restrict__ src, const int* __restrict__ dst,
                                                     const uint16_t* __restrict__ pos, const uint16_t* __restrict__ we, const uint16_t* __restrict__ wx,
                                                     const uint16_t* __restrict__ wh,
                                                     u32x2* __restrict__ R, uint32_t* __restrict__ T,
                                                     u32x2* __restrict__ pos4, u32x4* __restrict__ WF, int N, int E) {
    __shared__ __attribute__((aligned(16))) uint32_t hist[NBIN];
    __shared__ __attribute__((aligned(16))) u32x2 recS[CHUNK];
    const int tid = threadIdx.x, blk = blockIdx.x;
    const int e0 = blk * CHUNK;
    const int cnt = min(CHUNK, E - e0);
    STAMPDECL;
    STAMP(0, 0);
    if (tid < NBIN) hist[tid] = 0;
    int d[4], s[4];
#pragma unroll
    for (int r = 0; r < 4; ++r) { int i = r * 1024 + tid; d[r] = i < cnt ? dst[e0 + i] : 0; s[r] = i < cnt ? src[e0 + i] : 0; }
    for (int f = blk; f < 40; f += gridDim.x) {
        if (tid < 512) {
            bool ok;
            const uint16_t* p = wf_src(f, tid >> 3, tid & 7, we, wx, wh, ok);
            const uint16_t v = *p;
            ((uint16_t*)WF)[f * 512 + tid] = ok ? v : (uint16_t)0;
        }
    }
    for (int n = blk * BINSZ + tid - 512; tid >= 512 && tid < 512 + BINSZ && n < N; n += gridDim.x * BINSZ) {
        u32x2 p; p[0] = pos[n * 3] | ((uint32_t)pos[n * 3 + 1] << 16); p[1] = pos[n * 3 + 2]; pos4[n] = p;
    }
    STAMP(0, 1);
    __syncthreads();
    uint32_t bin[4], rank[4];
#pragma unroll
    for (int r = 0; r < 4; ++r) {
        int i = r * 1024 + tid;
        bin[r] = (uint32_t)d[r] / BINSZ;
        rank[r] = 0;
        if (i < cnt) rank[r] = atomicAdd(&hist[bin[r]], 1u);
    }
    __syncthreads();
    STAMP(0, 2);
    if (tid < 64) {
        u32x4 hv = *(const u32x4*)(hist + 4 * tid);
        const uint32_t sum = hv[0] + hv[1] + hv[2] + hv[3], x = wave_scan(sum);
        u32x4 st; st[0] = x - sum; st[1] = st[0] + hv[0]; st[2] = st[1] + hv[1]; st[3] = st[2] + hv[2];
        *(u32x4*)(hist + 4 * tid) = st;
#pragma unroll
        for (int i = 0; i < 4; ++i) T[(4 * tid + i) * NBLK + blk] = st[i] | (hv[i] << 16);
    }
    __syncthreads();
    STAMP(0, 3);
#pragma unroll
    for (int r = 0; r < 4; ++r) {
        int i = r * 1024 + tid;
        if (i < cnt) {
            const uint32_t dl = d[r] - bin[r] * BINSZ;
            u32x2 rec; rec[0] = (uint32_t)(e0 + i) | (dl << 24); rec[1] = (uint32_t)s[r] | (dl << 18);
            recS[hist[bin[r]] + rank[r]] = rec;
        }
    }
    __syncthreads();
    STAMP(0, 4);
#pragma unroll
    for (int r = 0; r < 2; ++r) {
        int i = r * 1024 + tid;
        if (2 * i < cnt) *(u32x4*)(R + e0 + 2 * i) = *(const u32x4*)(recS + 2 * i);
    }
    STAMP(0, 5);
    STAMP(0, 6);
    STAMPOUT(0);
}

DI void dma16(uint32_t ldsaddr, uint32_t voff, const void* base) {
    asm volatile("s_mov_b32 m0, %0\n\ts_nop 0\n\tglobal_load_lds_dwordx4 %1, %2" :: "s"(ldsaddr), "v"(voff), "s"(base) : "memory");
}
typedef __attribute__((address_space(3))) s16x4* lds_s16x4_p;
typedef __attribute__((address_space(3))) uint32_t* lds_u32_p;
DI s16x4 trread(uint32_t addr) { return __builtin_amdgcn_ds_read_tr16_b64_v4i16((lds_s16x4_p)(uintptr_t)addr); }
template <class T> DI T ldsld(uint32_t addr) { return *(__attribute__((address_space(3))) T*)(uintptr_t)addr; }
template <class T> DI void ldsst(uint32_t addr, T v) { *(__attribute__((address_space(3))) T*)(uintptr_t)addr = v; }
template <int V> struct IC { static constexpr int value = V; };

__global__ __launch_bounds__(512, 2) __attribute__((amdgpu_num_vgpr(120)))
void k2_main(const uint16_t* __restrict__ hg, const char* __restrict__ eag, const char* __restrict__ pos4g,
             const u32x2* __restrict__ R, const uint32_t* __restrict__ T,
             const u32x4* __restrict__ WF, uint16_t* __restrict__ out, int N, int nblk) {
    extern __shared__ __attribute__((aligned(16))) char ldsraw[];
    const uint32_t LB = (uint32_t)(uintptr_t)(__attribute__((address_space(3))) char*)ldsraw;
    const int tid = threadIdx.x, lane = tid & 63;
    const int w = __builtin_amdgcn_readfirstlane(tid >> 6);
    const int c = blockIdx.x;
    const int node0 = c * BINSZ;
    const int nnodes = min(BINSZ, N - node0);

    STAMPDECL;
    STAMP(1, 0);
    const uint32_t stg = LB + L_STG + w * STG_W;
#pragma unroll
    for (int i = 0; i < 3; ++i) dma16(stg + 4096 + i * 1024, (uint32_t)((3 * w + i) * 1024 + lane * 16), WF);
    u32x4 T4 = *(const u32x4*)(T + (size_t)c * NBLK + 4 * lane);
    if (tid < 324) ldsst<uint32_t>(LB + L_HIST + tid * 4, 0u);

    bf16x8 hb0[4];
    u32x2 pv0;
    {
        const int ng = min(node0 + 32 * w + (lane & 31), N - 1);
#pragma unroll
        for (int s = 0; s < 4; ++s) hb0[s] = *(const bf16x8*)(hg + (size_t)ng * 64 + 16 * s + 8 * (lane >> 5));
        pv0 = *(const u32x2*)(pos4g + (size_t)ng * 8);
        if (w == 7 && lane < 36) {
            const int k = lane >> 2, q = lane & 3;
            uint32_t v = (2 * q < k ? 0x3F80u : 0u) | (2 * q + 1 < k ? 0x3F800000u : 0u);
            ldsst<uint32_t>(LB + L_TAB + lane * 4, v);
        }
    }
    STAMP(1, 1);
    f32x16 Z0, Z1, Z2;
#pragma unroll
    for (int i = 0; i < 16; ++i) { Z0[i] = 0.f; Z1[i] = 0.f; Z2[i] = 0.f; }

    int nb0 = (w * 49) >> 1, nb1 = ((w + 1) * 49) >> 1;
    asm volatile("v_mov_b32 v248, 0\n\tv_mov_b32 v249, 0\n\tv_mov_b32 v250, 0\n\t"
                 "v_mov_b32 v252, 0\n\tv_mov_b32 v253, 0\n\tv_mov_b32 v254, 0" ::: ACLOB);

    int seg0 = 0, round = 0;
    bool first = true;
    for (;;) {
        int lq = __builtin_amdgcn_mbcnt_hi(~0u, __builtin_amdgcn_mbcnt_lo(~0u, 0u)); asm volatile("" : "+v"(lq));
        const int eq = lq & 31, hq = lq >> 5;
        if (!first) {
            T4 = *(const u32x4*)(T + (size_t)c * NBLK + 4 * lq);
            __syncthreads();
            if (w * 64 + lq < 324) ldsst<uint32_t>(LB + L_HIST + (w * 64 + lq) * 4, 0u);
#pragma unroll
            for (int i = 0; i < 3; ++i) dma16(stg + 4096 + i * 1024, (uint32_t)((3 * w + i) * 1024 + lq * 16), WF);
        }
        bool more = false;
        uint32_t sum = 0;
#pragma unroll
        for (int i = 0; i < 4; ++i) {
            const int sg = 4 * lq + i;
            int cn = (int)(T4[i] >> 16) - 32 * round;
            if (sg >= nblk) cn = 0;
            more = more || cn > 32;
            cn = min(max(cn, 0), 32);
            if (sg < seg0) cn = 0;
            T4[i] = cn > 0 ? (((T4[i] & 0xFFFF) + 32 * round) | ((uint32_t)cn << 16)) : 0u;
            sum += cn;
        }
        u32x4 ra[4], rb[4];
        int cn4[4];
        const int L = w * 8 + (lq >> 3);
#pragma unroll
        for (int p = 0; p < 4; ++p) {
            const uint32_t tv = __shfl(T4[p], L, 64);
            cn4[p] = (int)(tv >> 16) - 4 * (lq & 7);
            const u32x2* rp = R + (4 * L + p) * CHUNK + (tv & 0xFFFF) + (cn4[p] > 0 ? 4 * (lq & 7) : 0);
            ra[p] = *(const u32x4*)rp; rb[p] = *(const u32x4*)(rp + 2);
        }
        const uint32_t xs = wave_scan(sum);
        uint32_t n = __builtin_amdgcn_readlane(xs, 63);
        int seg1 = NBLK;
        if (n > (uint32_t)CAP) {
            const int lcut = __popcll(__ballot(xs <= (uint32_t)CAP));
            n = __builtin_amdgcn_readlane(xs, lcut - 1);
            seg1 = 4 * lcut;
            if (L >= lcut) { cn4[0] = 0; cn4[1] = 0; cn4[2] = 0; cn4[3] = 0; }
        }
        const bool anymore = __any(more);
        const bool single = first && seg1 == NBLK && !anymore;
        T4 = u32x4{0u, 0u, 0u, 0u};
        asm volatile("s_waitcnt vmcnt(8) lgkmcnt(0)" ::: "memory");
        __builtin_amdgcn_s_barrier();
        STAMP(1, 2);
#define WFL(f) (LB + L_STG + ((f) / 3) * STG_W + 4096 + ((f) % 3) * 1024)
        if (first) {
            f32x16 p0, p1;
#pragma unroll
            for (int i = 0; i < 16; ++i) { p0[i] = 0.f; p1[i] = 0.f; }
#pragma unroll
            for (int s = 0; s < 4; ++s) {
                p0 = MFMA32(ldsld<bf16x8>(WFL(16 + s) + lq * 16), hb0[s], p0);
                p1 = MFMA32(ldsld<bf16x8>(WFL(20 + s) + lq * 16), hb0[s], p1);
            }
            const int nl = 32 * w + eq;
            if (nl < BINSZ) {
                const uint32_t pa = LB + L_P + nl * PROW + 16 * hq;
#pragma unroll
                for (int g = 0; g < 4; ++g) {
                    f32x4 a, b;
#pragma unroll
                    for (int i = 0; i < 4; ++i) { a[i] = p0[4 * g + i]; b[i] = p1[4 * g + i]; }
                    ldsst<f32x4>(pa + 32 * g, a); ldsst<f32x4>(pa + 128 + 32 * g, b);
                }
                if (hq == 0) { f32x4 pd0; pd0[0] = bf2f(pv0[0] & 0xFFFF); pd0[1] = bf2f(pv0[0] >> 16); pd0[2] = bf2f(pv0[1] & 0xFFFF); pd0[3] = 0.f; ldsst<f32x4>(pa + 256, pd0); }
            }
        }
#pragma unroll
        for (int s = 0; s < 4; ++s) hb0[s] = bf16x8{0, 0, 0, 0, 0, 0, 0, 0};
        pv0 = u32x2{0u, 0u};
        bf16x8 W2[8], Wa[2], Wx[4];
        s16x4 Wd[2];
#pragma unroll
        for (int i = 0; i < 8; ++i) W2[i] = ldsld<bf16x8>(WFL(i) + lq * 16);
#pragma unroll
        for (int i = 0; i < 2; ++i) { Wa[i] = ldsld<bf16x8>(WFL(8 + i) + lq * 16); Wd[i] = ldsld<s16x4>(WFL(10 + i) + lq * 16); }
#pragma unroll
        for (int i = 0; i < 4; ++i) Wx[i] = ldsld<bf16x8>(WFL(12 + i) + lq * 16);
        uint32_t ha[16], rk[16];
        const uint32_t dummy = 1024 + 4 * lq;
#pragma unroll
        for (int p = 0; p < 4; ++p) {
#pragma unroll
            for (int i = 0; i < 4; ++i) {
                const uint32_t w1v = i == 0 ? ra[p][1] : i == 1 ? ra[p][3] : i == 2 ? rb[p][1] : rb[p][3];
                ha[4 * p + i] = cn4[p] > i ? (w1v >> 16) : dummy;
                rk[4 * p + i] = __hip_atomic_fetch_add((lds_u32_p)(uintptr_t)(LB + L_HIST + ha[4 * p + i]), 1u, __ATOMIC_RELAXED, __HIP_MEMORY_SCOPE_WORKGROUP);
            }
        }
        STAMP(1, 3);
        __syncthreads();
        {
            const u32x4 hv = ldsld<u32x4>(LB + L_HIST + lq * 16);
            const uint32_t sm = hv[0] + hv[1] + hv[2] + hv[3], x = wave_scan(sm);
            u32x4 st; st[0] = x - sm; st[1] = st[0] + hv[0]; st[2] = st[1] + hv[1]; st[3] = st[2] + hv[2];
            ldsst<u32x4>(stg + lq * 16, st);
            ldsst<uint32_t>(stg + dummy, (uint32_t)TRASH);
            if (single) {
                const uint32_t t0 = (n * w) >> 3, t1 = (n * (w + 1)) >> 3;
                int a0 = 0, a1 = 0;
#pragma unroll
                for (int i = 0; i < 4; ++i) { a0 += __popcll(__ballot(st[i] < t0)); a1 += __popcll(__ballot(st[i] < t1)); }
                if (w == 7) a1 = BINSZ;
                a0 = min(a0, BINSZ); a1 = min(a1, BINSZ);
                if (a1 - a0 > 31) ldsst<uint32_t>(LB + L_FLAG, 1u);
                nb0 |= a0 << 16; nb1 |= a1 << 16;
            }
        }
        STAMP(1, 4);
#pragma unroll
        for (int k = 0; k < 16; ++k) rk[k] += ldsld<uint32_t>(stg + ha[k]);
#pragma unroll
        for (int p = 0; p < 4; ++p) {
#pragma unroll
            for (int i = 0; i < 4; ++i) {
                const uint32_t w0v = i == 0 ? ra[p][0] : i == 1 ? ra[p][2] : i == 2 ? rb[p][0] : rb[p][2];
                const uint32_t w1v = i == 0 ? ra[p][1] : i == 1 ? ra[p][3] : i == 2 ? rb[p][1] : rb[p][3];
                ldsst<uint16_t>(LB + L_SRC + rk[4 * p + i] * 2, (uint16_t)w1v);
                ldsst<uint32_t>(LB + L_W1 + rk[4 * p + i] * 4, w0v);
            }
        }
        STAMP(1, 5);
        asm volatile("" :: "v"(W2[0]), "v"(W2[1]), "v"(W2[2]), "v"(W2[3]), "v"(W2[4]), "v"(W2[5]), "v"(W2[6]), "v"(W2[7]));
        asm volatile("" :: "v"(Wa[0]), "v"(Wa[1]), "v"(Wd[0]), "v"(Wd[1]), "v"(Wx[0]), "v"(Wx[1]), "v"(Wx[2]), "v"(Wx[3]));
        __syncthreads();
        STAMP(1, 6);
        if (single) {
            const bool bad = __builtin_amdgcn_readfirstlane(ldsld<uint32_t>(LB + L_FLAG)) != 0;
            nb0 = bad ? (nb0 & 0xFFFF) : (nb0 >> 16); nb1 = bad ? (nb1 & 0xFFFF) : (nb1 >> 16);
        }
        first = false;
        const bool last = seg1 == NBLK && !anymore;
        if (seg1 == NBLK) { seg0 = 0; ++round; } else seg0 = seg1;
        const int nn = nb1 - nb0;
        const uint32_t lov = ldsld<uint32_t>(stg + (nb0 + min(eq, nn)) * 4);
        const uint32_t eb = __builtin_amdgcn_readfirstlane(lov);
        const uint32_t ee = __builtin_amdgcn_readlane(lov, 31);
        const int cntw = (int)(ee - eb);
        const int nt = (cntw + 31) >> 5;
        int lo = 16 * ((int)lov - (int)eb - 8 * hq);
        STAMP(1, 7);
        if (nt == 0) { if (last) break; continue; }

        uint32_t hfa[4];
#pragma unroll
        for (int s = 0; s < 4; ++s) hfa[s] = stg + eq * 128 + (((2 * s + hq) ^ ((eq >> 1) & 7)) << 4);
        const uint32_t lp0 = ((lq & 7) ^ ((lq >> 4) & 7)) << 4;
        const uint32_t lp1 = ((lq & 7) ^ ((4 + (lq >> 4)) & 7)) << 4;
        const uint32_t mbuf = stg + 8192;
        const uint32_t mwa = mbuf + eq * 72 + 8 * hq;
        const uint32_t tra = mbuf + (8 * hq + ((lq & 15) >> 2)) * 72 + ((lq >> 4) & 1) * 32 + (lq & 3) * 8;
        const uint32_t vwa = mbuf + lq * 8;
        const uint32_t vra = mbuf + (8 * hq + ((lq & 15) >> 2)) * 8;

        uint32_t w1A = 0, w1B = 0;
        uint32_t ix0, ix1, ix2, ix3, ixm;
        auto pf_idx = [&](int t, uint32_t& w1X) {
            const uint32_t li = eb + 32 * t, le = ee - 1;
            const uint32_t i8 = li + (lq >> 3);
            ix0 = ldsld<uint16_t>(LB + L_SRC + 2 * min(i8, le)); ix1 = ldsld<uint16_t>(LB + L_SRC + 2 * min(i8 + 8, le));
            ix2 = ldsld<uint16_t>(LB + L_SRC + 2 * min(i8 + 16, le)); ix3 = ldsld<uint16_t>(LB + L_SRC + 2 * min(i8 + 24, le));
            const uint32_t ie = min(li + eq, le);
            ixm = ldsld<uint16_t>(LB + L_SRC + 2 * ie);
            w1X = ldsld<uint32_t>(LB + L_W1 + 4 * ie);
        };
        auto pf_dma = [&](auto SET, int q, uint32_t ix) { dma16(stg + decltype(SET)::value * 4096 + q * 1024, (HM(ix) << 7) | ((q & 1) ? lp1 : lp0), hg); };
        auto pf_ea = [&](auto SET, uint32_t w1) {
            constexpr int AE = 240 + decltype(SET)::value * 4;
            const uint32_t eo = ((HE(w1) << 5) & 0x1FFFFFE0u) | (16 * hq);
            const char* bp = eag;
            asm volatile("global_load_dwordx4 v[%c2:%c3], %0, %1 " EAPOL :: "v"(eo), "s"(bp), "i"(AE), "i"(AE + 3) : "memory", ACLOB);
        };
        auto pf_pos = [&](auto SET) {
            constexpr int AP = 248 + decltype(SET)::value * 4;
            const char* bp = pos4g; const uint32_t po = HM(ixm) << 3;
            asm volatile("s_mov_b32 exec_hi, 0\n\tglobal_load_dwordx2 v[%c2:%c3], %0, %1\n\ts_mov_b32 exec_hi, -1" :: "v"(po), "s"(bp), "i"(AP), "i"(AP + 1) : "memory", ACLOB);
        };
        auto prefetch = [&](auto SET, int t, uint32_t& w1X) {
            pf_idx(t, w1X);
            pf_dma(SET, 0, ix0); pf_dma(SET, 1, ix1); pf_dma(SET, 2, ix2); pf_dma(SET, 3, ix3);
            pf_ea(SET, w1X); pf_pos(SET);
        };

        f32x16 x0, x1;
        bf16x8 hf[4];
        s16x4 d2v;
        float dxc, dyc, dzc;
        float dxp = 0.f, dyp = 0.f, dzp = 0.f;
        uint32_t pac;
        uint32_t m[8];
#pragma unroll
        for (int i = 0; i < 8; ++i) m[i] = 0u;
        f32x4 cs = {0.f, 0.f, 0.f, 0.f};
        u32x4 S0 = {0u, 0u, 0u, 0u}, S1 = {0u, 0u, 0u, 0u};
        bf16x8 A0, A1;
#pragma unroll
        for (int i = 0; i < 8; ++i) { A0[i] = 0; A1[i] = 0; }
        f32x4 pd;

        auto ldP = [&](f32x16& x, uint32_t a) {
#pragma unroll
            for (int g = 0; g < 4; ++g) {
                const f32x4 v = ldsld<f32x4>(a + 32 * g);
#pragma unroll
                for (int i = 0; i < 4; ++i) x[4 * g + i] = v[i];
            }
        };
        auto load_tile = [&](auto SET, uint32_t w1X) {
            const uint32_t prow = LB + L_P + (w1X >> 24) * PROW;
            pac = prow + 16 * hq;
            pd = ldsld<f32x4>(prow + 256);
            ldP(x0, pac);
#pragma unroll
            for (int s = 0; s < 4; ++s) hf[s] = ldsld<bf16x8>(hfa[s] + decltype(SET)::value * 4096);
        };
        auto mkdiff = [&](auto SET) {
            constexpr int AP = 248 + decltype(SET)::value * 4;
            const float p0 = pd[0], p1 = pd[1], p2 = pd[2]; float q0, q1, q2;
            asm volatile("v_lshlrev_b32 %0, 16, v%c3\n\tv_and_b32 %1, 0xffff0000, v%c3\n\tv_lshlrev_b32 %2, 16, v%c4"
                         : "=&v"(q0), "=&v"(q1), "=&v"(q2) : "i"(AP), "i"(AP + 1));
            dxc = p0 - q0; dyc = p1 - q1; dzc = p2 - q2;
            const float d2 = dxc * dxc + dyc * dyc + dzc * dzc;
            u32x2 dv; dv[0] = pkbf(d2, d2); dv[1] = dv[0];
            d2v = __builtin_bit_cast(s16x4, dv);
        };
        auto posupd = [&]() {
            const uint32_t c0 = __float_as_uint(cs[0]);
            const auto sw = __builtin_amdgcn_permlane32_swap(c0, c0, false, false);
            const float cv = __uint_as_float(sw[0]) + __uint_as_float(sw[1]);
            u32x2 vv; vv[0] = pkbf(dxp * cv, dyp * cv); vv[1] = pkbf(dzp * cv, dzp);
            ldsst<u32x2>(vwa, vv);
            const s16x4 a0 = trread(vra), a1 = trread(vra + 32), a2 = trread(vra + 128), a3 = trread(vra + 160);
            A0 = __builtin_shufflevector(a0, a1, 0, 1, 2, 3, 4, 5, 6, 7); A1 = __builtin_shufflevector(a2, a3, 0, 1, 2, 3, 4, 5, 6, 7);
            cs = f32x4{0.f, 0.f, 0.f, 0.f};
            dxp = dxc; dyp = dyc; dzp = dzc;
        };
        auto ea_mfma = [&](auto SET, f32x16& x, const bf16x8& wa) {
            constexpr int AE = 240 + decltype(SET)::value * 4;
            asm volatile("v_mfma_f32_32x32x16_bf16 %0, %1, v[%c2:%c3], %0" : "+v"(x) : "v"(wa), "i"(AE), "i"(AE + 3));
        };
        auto S_read = [&]() {
            const uint32_t ta0 = (uint32_t)min(max(lo, 0), 128), ta1 = (uint32_t)min(max(lo - 256, 0), 128);
            lo -= 512;
            S0 = ldsld<u32x4>(LB + L_TAB + ta0); S1 = ldsld<u32x4>(LB + L_TAB + ta1);
        };
        auto S_xor = [&]() {
#pragma unroll
            for (int i = 0; i < 4; ++i) {
                S0[i] ^= (uint32_t)__builtin_amdgcn_update_dpp(0, (int)S0[i], 0x130, 0xF, 0xF, false);
                S1[i] ^= (uint32_t)__builtin_amdgcn_update_dpp(0, (int)S1[i], 0x130, 0xF, 0xF, false);
            }
            asm volatile("" : "+v"(S0), "+v"(S1));
        };
        auto mwrite_tr = [&]() {
#pragma unroll
            for (int g = 0; g < 4; ++g) { u32x2 v; v[0] = m[2 * g]; v[1] = m[2 * g + 1]; ldsst<u32x2>(mwa + 16 * g, v); }
            const s16x4 a0 = trread(tra), a1 = trread(tra + 288), a2 = trread(tra + 1152), a3 = trread(tra + 1440);
            A0 = __builtin_shufflevector(a0, a1, 0, 1, 2, 3, 4, 5, 6, 7); A1 = __builtin_shufflevector(a2, a3, 0, 1, 2, 3, 4, 5, 6, 7);
        };
        auto csq = [&](int fb, int q) {
            const bf16x8& wv = Wx[2 * fb + (q >> 1)];
            const s16x4 a = (q & 1) ? __builtin_shufflevector(wv, wv, 4, 5, 6, 7) : __builtin_shufflevector(wv, wv, 0, 1, 2, 3);
            u32x2 b; b[0] = m[2 * q]; b[1] = m[2 * q + 1];
            cs = MFMA4(a, __builtin_bit_cast(s16x4, b), cs);
        };
#define SILU4(x, b, mo, SLOT1, SLOT2, SLOT3) do { \
            float t0 = x[4 * b] * NLOG2E, t1 = x[4 * b + 1] * NLOG2E, t2 = x[4 * b + 2] * NLOG2E, t3 = x[4 * b + 3] * NLOG2E; SB(); \
            SLOT1; SB(); \
            t0 = __builtin_amdgcn_exp2f(t0); t1 = __builtin_amdgcn_exp2f(t1); t2 = __builtin_amdgcn_exp2f(t2); t3 = __builtin_amdgcn_exp2f(t3); SB(); \
            SLOT2; SB(); \
            t0 += 1.f; t1 += 1.f; t2 += 1.f; t3 += 1.f; SB(); \
            t0 = __builtin_amdgcn_rcpf(t0); t1 = __builtin_amdgcn_rcpf(t1); t2 = __builtin_amdgcn_rcpf(t2); t3 = __builtin_amdgcn_rcpf(t3); SB(); \
            SLOT3; SB(); \
            t0 *= x[4 * b]; t1 *= x[4 * b + 1]; t2 *= x[4 * b + 2]; t3 *= x[4 * b + 3]; SB(); \
            mo[2 * b] = pkbf(t0, t1); mo[2 * b + 1] = pkbf(t2, t3); asm volatile("" : "+v"(mo[2 * b]), "+v"(mo[2 * b + 1])); SB(); } while (0)

        auto body = [&](auto SET, auto NXT, int t, uint32_t& w1X, uint32_t& w1N) {
            uint32_t mn[8];
            ldP(x1, pac + 128);
            pf_idx(t + 2, w1X);
            SB();
            SILU4(x0, 0, mn, (csq(1, 0), csq(1, 1)), (csq(1, 2), csq(1, 3)), Z1 = MFMA32(A0, BS(S0), Z1));
            SILU4(x0, 1, mn, Z1 = MFMA32(A1, BS(S1), Z1), ea_mfma(SET, x1, Wa[1]), (x1 = MFMA8(Wd[1], d2v, x1), posupd()));
            SILU4(x0, 2, mn, (x1 = MFMA32(W2[4], hf[0], x1), pf_dma(SET, 0, ix0)), (x1 = MFMA32(W2[5], hf[1], x1), pf_dma(SET, 1, ix1)), (x1 = MFMA32(W2[6], hf[2], x1), pf_dma(SET, 2, ix2)));
            SILU4(x0, 3, mn, (x1 = MFMA32(W2[7], hf[3], x1), pf_dma(SET, 3, ix3)), (Z2 = MFMA32(A0, BS(S0), Z2), pf_ea(SET, w1X)), (Z2 = MFMA32(A1, BS(S1), Z2), pf_pos(SET)));
#pragma unroll
            for (int i = 0; i < 8; ++i) m[i] = mn[i];
            mwrite_tr();
            S_read();
            asm volatile("s_waitcnt vmcnt(6)" ::: "memory");
            load_tile(NXT, w1N);
            SB();
            SILU4(x1, 0, mn, (csq(0, 0), csq(0, 1)), (csq(0, 2), csq(0, 3), S_xor()), Z0 = MFMA32(A0, BS(S0), Z0));
            SILU4(x1, 1, mn, (Z0 = MFMA32(A1, BS(S1), Z0), mkdiff(NXT)), ea_mfma(NXT, x0, Wa[0]), x0 = MFMA8(Wd[0], d2v, x0));
            SILU4(x1, 2, mn, x0 = MFMA32(W2[0], hf[0], x0), x0 = MFMA32(W2[1], hf[1], x0), x0 = MFMA32(W2[2], hf[2], x0));
            SILU4(x1, 3, mn, x0 = MFMA32(W2[3], hf[3], x0), (void)0, (void)0);
#pragma unroll
            for (int i = 0; i < 8; ++i) m[i] = mn[i];
            mwrite_tr();
            SB();
        };

        prefetch(IC<0>{}, 0, w1A);
        prefetch(IC<1>{}, 1, w1B);
        asm volatile("s_waitcnt vmcnt(6)" ::: "memory");
        load_tile(IC<0>{}, w1A);
        mkdiff(IC<0>{});
        ea_mfma(IC<0>{}, x0, Wa[0]);
        x0 = MFMA8(Wd[0], d2v, x0);
#pragma unroll
        for (int s = 0; s < 4; ++s) x0 = MFMA32(W2[s], hf[s], x0);
#pragma unroll 1
        for (int t = 0; t < nt; t += 2) {
            body(IC<0>{}, IC<1>{}, t, w1A, w1B);
            if (t + 1 < nt) body(IC<1>{}, IC<0>{}, t + 1, w1B, w1A);
        }
        asm volatile("s_waitcnt vmcnt(0)" ::: "memory");
        csq(1, 0); csq(1, 1); csq(1, 2); csq(1, 3);
        Z1 = MFMA32(A0, BS(S0), Z1);
        Z1 = MFMA32(A1, BS(S1), Z1);
        posupd();
        Z2 = MFMA32(A0, BS(S0), Z2);
        Z2 = MFMA32(A1, BS(S1), Z2);
        if (last) break;
    }

    STAMP(1, 8);
    {
        int lz = __builtin_amdgcn_mbcnt_hi(~0u, __builtin_amdgcn_mbcnt_lo(~0u, 0u)); asm volatile("" : "+v"(lz));
        const int ez = lz & 31, hz = lz >> 5;
        const int nn = nb1 - nb0;
        const int nl = nb0 + ez;
        const bool valid = ez < nn && nl < nnodes;
        const int ng = min(node0 + nl, N - 1);
        bf16x8 hb[4];
#pragma unroll
        for (int s = 0; s < 4; ++s) hb[s] = *(const bf16x8*)(hg + (size_t)ng * 64 + 16 * s + 8 * hz);
        u32x2 hv[8];
#pragma unroll
        for (int i = 0; i < 8; ++i) hv[i] = *(const u32x2*)(hg + (size_t)ng * 64 + 8 * i + 4 * hz);
        bf16x8 wf[16];
#pragma unroll
        for (int i = 0; i < 16; ++i) wf[i] = BS(WF[(24 + i) * 64 + lz]);
        u32x4 zb[4];
#pragma unroll
        for (int i = 0; i < 4; ++i) {
            zb[0][i] = pkbf(Z0[2 * i], Z0[2 * i + 1]); zb[1][i] = pkbf(Z0[8 + 2 * i], Z0[8 + 2 * i + 1]);
            zb[2][i] = pkbf(Z1[2 * i], Z1[2 * i + 1]); zb[3][i] = pkbf(Z1[8 + 2 * i], Z1[8 + 2 * i + 1]);
        }
#pragma unroll
        for (int ob = 0; ob < 2; ++ob) {
            f32x16 o;
#pragma unroll
            for (int i = 0; i < 16; ++i) o[i] = 0.f;
#pragma unroll
            for (int s = 0; s < 4; ++s) o = MFMA32(wf[ob * 4 + s], hb[s], o);
#pragma unroll
            for (int s = 0; s < 4; ++s) o = MFMA32(wf[8 + ob * 4 + s], BS(zb[s]), o);
            if (valid) {
#pragma unroll
                for (int g = 0; g < 4; ++g) {
                    const size_t off = (size_t)ng * 64 + 32 * ob + 8 * g + 4 * hz;
                    const u32x2 v = hv[4 * ob + g];
                    u32x2 r;
                    r[0] = pkbf(bf2f(v[0] & 0xFFFF) + silu1(o[4 * g]), bf2f(v[0] >> 16) + silu1(o[4 * g + 1]));
                    r[1] = pkbf(bf2f(v[1] & 0xFFFF) + silu1(o[4 * g + 2]), bf2f(v[1] >> 16) + silu1(o[4 * g + 3]));
                    *(u32x2*)(out + off) = r;
                }
            }
        }
        if (valid && hz == 0) {
            const uint32_t prow = LB + L_P + nl * PROW;
            const f32x4 pd = ldsld<f32x4>(prow + 256);
            uint16_t* po = out + (size_t)N * 64 + (size_t)ng * 3;
            po[0] = f2bf(pd[0] + Z2[0]); po[1] = f2bf(pd[1] + Z2[1]); po[2] = f2bf(pd[2] + Z2[2]);
        }
    }
    STAMP(1, 9);
    STAMPOUT(1);
}

static DI float rbf(float f) { return bf2f(f2bf(f)); }
__global__ void fb_zero(float* p, int n) { int i = blockIdx.x * 256 + threadIdx.x; if (i < n) p[i] = 0.f; }
__global__ void fb_edge(const uint16_t* h, const uint16_t* pos, const uint16_t* ea, const int* src, const int* dst,
                        const uint16_t* we, const uint16_t* wx, float* mi, float* pa, int E) {
    int e = blockIdx.x * 4 + (threadIdx.x >> 6);
    int f = threadIdx.x & 63;
    if (e >= E) return;
    size_t s = src[e], d = dst[e];
    float df0 = bf2f(pos[d * 3]) - bf2f(pos[s * 3]), df1 = bf2f(pos[d * 3 + 1]) - bf2f(pos[s * 3 + 1]), df2 = bf2f(pos[d * 3 + 2]) - bf2f(pos[s * 3 + 2]);
    float d2 = rbf(df0 * df0 + df1 * df1 + df2 * df2);
    float acc = 0.f;
    for (int k = 0; k < 64; ++k) acc += bf2f(h[d * 64 + k]) * bf2f(we[k * 64 + f]);
    for (int k = 0; k < 64; ++k) acc += bf2f(h[s * 64 + k]) * bf2f(we[(64 + k) * 64 + f]);
    acc += d2 * bf2f(we[128 * 64 + f]);
    for (int k = 0; k < 16; ++k) acc += bf2f(ea[(size_t)e * 16 + k]) * bf2f(we[(129 + k) * 64 + f]);
    float m = rbf(silu1(acc));
    float cs = m * bf2f(wx[f]);
    for (int o = 32; o > 0; o >>= 1) cs += __shfl_xor(cs, o, 64);
    atomicAdd(&mi[d * 64 + f], m);
    if (f < 3) atomicAdd(&pa[d * 3 + f], (f == 0 ? df0 : f == 1 ? df1 : df2) * cs);
}
__global__ void fb_node(const uint16_t* h, const uint16_t* pos, const uint16_t* wh, const float* mi, const float* pa, uint16_t* out, int N) {
    int n = blockIdx.x * 4 + (threadIdx.x >> 6);
    int f = threadIdx.x & 63;
    if (n >= N) return;
    float acc = 0.f;
    for (int k = 0; k < 64; ++k) acc += bf2f(h[(size_t)n * 64 + k]) * bf2f(wh[k * 64 + f]);
    for (int k = 0; k < 64; ++k) acc += rbf(mi[(size_t)n * 64 + k]) * bf2f(wh[(64 + k) * 64 + f]);
    out[(size_t)n * 64 + f] = f2bf(bf2f(h[(size_t)n * 64 + f]) + silu1(acc));
    if (f < 3) out[(size_t)N * 64 + (size_t)n * 3 + f] = f2bf(bf2f(pos[(size_t)n * 3 + f]) + pa[(size_t)n * 3 + f]);
}

extern "C" void kernel_launch(void* const* d_in, const int* in_sizes, int n_in,
                              void* d_out, int out_size, void* d_ws, size_t ws_size,
                              hipStream_t stream) {
    const uint16_t* h = (const uint16_t*)d_in[0];
    const uint16_t* pos = (const uint16_t*)d_in[1];
    const uint16_t* ea = (const uint16_t*)d_in[2];
    const int* src = (const int*)d_in[3];
    const int* dst = (const int*)d_in[4];
    const uint16_t* we = (const uint16_t*)d_in[5];
    const uint16_t* wx = (const uint16_t*)d_in[6];
    const uint16_t* wh = (const uint16_t*)d_in[7];
    const int N = in_sizes[0] / 64, E = in_sizes[3];
    const int nbin = (N + BINSZ - 1) / BINSZ, nblk = (E + CHUNK - 1) / CHUNK;
    if (nbin > NBIN || nblk > NBLK || N < 1 || E < 1) {
        float* mi = (float*)d_ws; float* pa = mi + (size_t)N * 64;
        int nz = N * 67;
        fb_zero<<<(nz + 255) / 256, 256, 0, stream>>>(mi, nz);
        if (E > 0) fb_edge<<<(E + 3) / 4, 256, 0, stream>>>(h, pos, ea, src, dst, we, wx, mi, pa, E);
        fb_node<<<(N + 3) / 4, 256, 0, stream>>>(h, pos, wh, mi, pa, (uint16_t*)d_out, N);
        return;
    }
    char* ws = (char*)d_ws;
    uint32_t* T = (uint32_t*)ws;
    u32x4* WF = (u32x4*)(ws + 262144);
    u32x2* pos4 = (u32x2*)(ws + 327680);
    u32x2* R = (u32x2*)(ws + 327680 + 802816);
    (void)hipFuncSetAttribute((const void*)k2_main, hipFuncAttributeMaxDynamicSharedMemorySize, L_TOTAL);
    k1_partition<<<nblk, 1024, 0, stream>>>(src, dst, pos, we, wx, wh, R, T, pos4, WF, N, E);
    k2_main<<<nbin, 512, L_TOTAL, stream>>>(h, (const char*)ea, (const char*)pos4, R, T, WF, (uint16_t*)d_out, N, nblk);
}
```

```cpp
#include <hip/hip_runtime.h>
#include <hip/hip_bf16.h>
#include <stdint.h>

using u32x4 = __attribute__((ext_vector_type(4))) unsigned;
using u32x2 = __attribute__((ext_vector_type(2))) unsigned;
using f32x16 = __attribute__((ext_vector_type(16))) float;
using f32x4 = __attribute__((ext_vector_type(4))) float;
using f32x2 = __attribute__((ext_vector_type(2))) float;
using bf16x8 = __attribute__((ext_vector_type(8))) short;
using s16x4 = __attribute__((ext_vector_type(4))) short;
using bf16x2 = __attribute__((ext_vector_type(2))) __bf16;
#define DI __device__ __forceinline__
#define ACLOB "v240", "v241", "v242", "v243", "v244", "v245", "v246", "v247", "v248", "v249", "v250", "v251", "v252", "v253", "v254", "v255"
#define MFMA32(a, b, c) __builtin_amdgcn_mfma_f32_32x32x16_bf16((a), (b), (c), 0, 0, 0)
#define MFMA8(a, b, c) __builtin_amdgcn_mfma_f32_32x32x8bf16_1k((a), (b), (c), 0, 0, 0)
#define MFMA4(a, b, c) __builtin_amdgcn_mfma_f32_4x4x4bf16_1k((a), (b), (c), 0, 0, 0)
#define SB() __builtin_amdgcn_sched_barrier(0)
#define BS(x) __builtin_bit_cast(bf16x8, x)

constexpr int CHUNK = 3136;
constexpr int BINSZ = 196;
constexpr int NBIN = 256;
constexpr int NBLK = 256;
constexpr int CAP = 3392;
constexpr int TRASH = CAP + 64;
constexpr int LISTN = TRASH + 136;

constexpr int PROW = 272;
constexpr int L_P = 0;
constexpr int L_SRC = BINSZ * PROW;
constexpr int L_W1 = L_SRC + LISTN * 2;
constexpr int L_TAB = L_W1 + LISTN * 4;
constexpr int L_HIST = L_TAB + 160;
constexpr int L_FLAG = L_HIST + 320 * 4;
constexpr int L_STG = ((L_FLAG + 16 + 127) / 128) * 128;
constexpr int STG_W = 8192 + 2304;
constexpr int L_TOTAL = L_STG + 8 * STG_W;
static_assert(L_TOTAL <= 163840, "LDS");
static_assert(L_W1 % 16 == 0 && L_TAB % 16 == 0 && L_HIST % 16 == 0, "align");
constexpr float NLOG2E = -1.44269504089f;

DI float bf2f(uint32_t v) { return __uint_as_float(v << 16); }
DI uint32_t pkbf(float a, float b) { bf16x2 v; v[0] = (__bf16)a; v[1] = (__bf16)b; return __builtin_bit_cast(uint32_t, v); }
DI uint16_t f2bf(float a) { __bf16 v = (__bf16)a; return __builtin_bit_cast(uint16_t, v); }
DI float silu1(float x) { return x * __builtin_amdgcn_rcpf(1.f + __builtin_amdgcn_exp2f(x * NLOG2E)); }

DI uint32_t wave_scan(uint32_t x) {
    x += (uint32_t)__builtin_amdgcn_update_dpp(0, (int)x, 0x111, 0xF, 0xF, false);
    x += (uint32_t)__builtin_amdgcn_update_dpp(0, (int)x, 0x112, 0xF, 0xF, false);
    x += (uint32_t)__builtin_amdgcn_update_dpp(0, (int)x, 0x114, 0xF, 0xF, false);
    x += (uint32_t)__builtin_amdgcn_update_dpp(0, (int)x, 0x118, 0xF, 0xF, false);
    x += (uint32_t)__builtin_amdgcn_update_dpp(0, (int)x, 0x142, 0xA, 0xF, false);
    x += (uint32_t)__builtin_amdgcn_update_dpp(0, (int)x, 0x143, 0xC, 0xF, false);
    return x;
}

#define STAMPDECL
#define STAMP(K, i)
#define STAMPOUT(K)
#define EAPOL ""
#define HM(x) (x)
#define HE(x) (x)

DI const uint16_t* wf_src(int f, int l, int j, const uint16_t* we, const uint16_t* wx, const uint16_t* wh, bool& ok) {
    const int r = l & 31, h = l >> 5;
    const int kn = 8 * h + j, kp = 8 * (j >> 2) + 4 * h + (j & 3);
    ok = true;
    if (f < 8) return we + (64 + 16 * (f & 3) + kn) * 64 + 32 * (f >> 2) + r;
    if (f < 10) return we + (129 + kn) * 64 + 32 * (f - 8) + r;
    if (f < 12) { ok = kn == 0; return we + 128 * 64 + 32 * (f - 10) + r; }
    if (f < 16) return wx + 32 * ((f - 12) >> 1) + 16 * ((f - 12) & 1) + kp;
    if (f < 24) return we + (16 * ((f - 16) & 3) + kn) * 64 + 32 * ((f - 16) >> 2) + r;
    if (f < 32) return wh + (16 * ((f - 24) & 3) + kn) * 64 + 32 * ((f - 24) >> 2) + r;
    return wh + (64 + 32 * (((f - 32) >> 1) & 1) + 16 * ((f - 32) & 1) + kp) * 64 + 32 * ((f - 32) >> 2) + r;
}

__global__ __launch_bounds__(1024) void k1_partition(const int* __restrict__ src, const int* __restrict__ dst,
                                                     const uint16_t* __restrict__ pos, const uint16_t* __restrict__ we, const uint16_t* __restrict__ wx,
                                                     const uint16_t* __restrict__ wh,
                                                     u32x2* __restrict__ R, uint32_t* __restrict__ T,
                                                     u32x2* __restrict__ pos4, u32x4* __restrict__ WF, int N, int E) {
    __shared__ __attribute__((aligned(16))) uint32_t hist[NBIN];
    __shared__ __attribute__((aligned(16))) u32x2 recS[CHUNK];
    const int tid = threadIdx.x, blk = blockIdx.x;
    const int e0 = blk * CHUNK;
    const int cnt = min(CHUNK, E - e0);
    STAMPDECL;
    STAMP(0, 0);
    if (tid < NBIN) hist[tid] = 0;
    int d[4], s[4];
#pragma unroll
    for (int r = 0; r < 4; ++r) { int i = r * 1024 + tid; d[r] = i < cnt ? dst[e0 + i] : 0; s[r] = i < cnt ? src[e0 + i] : 0; }
    unsigned cpf = 0;
    {
        unsigned long long pc; unsigned sz;
        asm volatile("s_getpc_b64 %0\n.Lcpf_a:\n\ts_mov_b32 %1, .Lcpf_z-.Lcpf_a" : "=s"(pc), "=s"(sz));
        if (blk < 16 && (unsigned)tid * 64u + 4u <= sz) cpf = *(const __attribute__((address_space(1))) unsigned*)(pc + (unsigned)tid * 64u);
    }
    for (int f = blk; f < 40; f += gridDim.x) {
        if (tid < 512) {
            bool ok;
            const uint16_t* p = wf_src(f, tid >> 3, tid & 7, we, wx, wh, ok);
            const uint16_t v = *p;
            ((uint16_t*)WF)[f * 512 + tid] = ok ? v : (uint16_t)0;
        }
    }
    for (int n = blk * BINSZ + tid - 512; tid >= 512 && tid < 512 + BINSZ && n < N; n += gridDim.x * BINSZ) {
        u32x2 p; p[0] = pos[n * 3] | ((uint32_t)pos[n * 3 + 1] << 16); p[1] = pos[n * 3 + 2]; pos4[n] = p;
    }
    STAMP(0, 1);
    __syncthreads();
    uint32_t bin[4], rank[4];
#pragma unroll
    for (int r = 0; r < 4; ++r) {
        int i = r * 1024 + tid;
        bin[r] = (uint32_t)d[r] / BINSZ;
        rank[r] = 0;
        if (i < cnt) rank[r] = atomicAdd(&hist[bin[r]], 1u);
    }
    __syncthreads();
    STAMP(0, 2);
    if (tid < 64) {
        u32x4 hv = *(const u32x4*)(hist + 4 * tid);
        const uint32_t sum = hv[0] + hv[1] + hv[2] + hv[3], x = wave_scan(sum);
        u32x4 st; st[0] = x - sum; st[1] = st[0] + hv[0]; st[2] = st[1] + hv[1]; st[3] = st[2] + hv[2];
        *(u32x4*)(hist + 4 * tid) = st;
#pragma unroll
        for (int i = 0; i < 4; ++i) T[(4 * tid + i) * NBLK + blk] = st[i] | (hv[i] << 16);
    }
    __syncthreads();
    STAMP(0, 3);
#pragma unroll
    for (int r = 0; r < 4; ++r) {
        int i = r * 1024 + tid;
        if (i < cnt) {
            const uint32_t dl = d[r] - bin[r] * BINSZ;
            u32x2 rec; rec[0] = (uint32_t)(e0 + i) | (dl << 24); rec[1] = (uint32_t)s[r] | (dl << 18);
            recS[hist[bin[r]] + rank[r]] = rec;
        }
    }
    __syncthreads();
    STAMP(0, 4);
#pragma unroll
    for (int r = 0; r < 2; ++r) {
        int i = r * 1024 + tid;
        if (2 * i < cnt) *(u32x4*)(R + e0 + 2 * i) = *(const u32x4*)(recS + 2 * i);
    }
    STAMP(0, 5);
    asm volatile("" :: "v"(cpf));
    STAMP(0, 6);
    STAMPOUT(0);
}

DI void dma16(uint32_t ldsaddr, uint32_t voff, const void* base) {
    asm volatile("s_mov_b32 m0, %0\n\ts_nop 0\n\tglobal_load_lds_dwordx4 %1, %2" :: "s"(ldsaddr), "v"(voff), "s"(base) : "memory");
}
typedef __attribute__((address_space(3))) s16x4* lds_s16x4_p;
typedef __attribute__((address_space(3))) uint32_t* lds_u32_p;
DI s16x4 trread(uint32_t addr) { return __builtin_amdgcn_ds_read_tr16_b64_v4i16((lds_s16x4_p)(uintptr_t)addr); }
template <class T> DI T ldsld(uint32_t addr) { return *(__attribute__((address_space(3))) T*)(uintptr_t)addr; }
template <class T> DI void ldsst(uint32_t addr, T v) { *(__attribute__((address_space(3))) T*)(uintptr_t)addr = v; }
template <int V> struct IC { static constexpr int value = V; };

__global__ __launch_bounds__(512, 2) __attribute__((amdgpu_num_vgpr(120)))
void k2_main(const uint16_t* __restrict__ hg, const char* __restrict__ eag, const char* __restrict__ pos4g,
             const u32x2* __restrict__ R, const uint32_t* __restrict__ T,
             const u32x4* __restrict__ WF, uint16_t* __restrict__ out, int N, int nblk) {
    extern __shared__ __attribute__((aligned(16))) char ldsraw[];
    const uint32_t LB = (uint32_t)(uintptr_t)(__attribute__((address_space(3))) char*)ldsraw;
    const int tid = threadIdx.x, lane = tid & 63;
    const int w = __builtin_amdgcn_readfirstlane(tid >> 6);
    const int c = blockIdx.x;
    const int node0 = c * BINSZ;
    const int nnodes = min(BINSZ, N - node0);

    STAMPDECL;
    STAMP(1, 0);
    const uint32_t stg = LB + L_STG + w * STG_W;
#pragma unroll
    for (int i = 0; i < 3; ++i) dma16(stg + 4096 + i * 1024, (uint32_t)((3 * w + i) * 1024 + lane * 16), WF);
    u32x4 T4 = *(const u32x4*)(T + (size_t)c * NBLK + 4 * lane);
    if (tid < 324) ldsst<uint32_t>(LB + L_HIST + tid * 4, 0u);

    bf16x8 hb0[4];
    u32x2 pv0;
    {
        const int ng = min(node0 + 32 * w + (lane & 31), N - 1);
#pragma unroll
        for (int s = 0; s < 4; ++s) hb0[s] = *(const bf16x8*)(hg + (size_t)ng * 64 + 16 * s + 8 * (lane >> 5));
        pv0 = *(const u32x2*)(pos4g + (size_t)ng * 8);
        if (w == 7 && lane < 36) {
            const int k = lane >> 2, q = lane & 3;
            uint32_t v = (2 * q < k ? 0x3F80u : 0u) | (2 * q + 1 < k ? 0x3F800000u : 0u);
            ldsst<uint32_t>(LB + L_TAB + lane * 4, v);
        }
    }
    STAMP(1, 1);
    f32x16 Z0, Z1, Z2;
#pragma unroll
    for (int i = 0; i < 16; ++i) { Z0[i] = 0.f; Z1[i] = 0.f; Z2[i] = 0.f; }

    int nb0 = (w * 49) >> 1, nb1 = ((w + 1) * 49) >> 1;
    asm volatile("v_mov_b32 v248, 0\n\tv_mov_b32 v249, 0\n\tv_mov_b32 v250, 0\n\t"
                 "v_mov_b32 v252, 0\n\tv_mov_b32 v253, 0\n\tv_mov_b32 v254, 0" ::: ACLOB);

    int seg0 = 0, round = 0;
    bool first = true;
    for (;;) {
        int lq = __builtin_amdgcn_mbcnt_hi(~0u, __builtin_amdgcn_mbcnt_lo(~0u, 0u)); asm volatile("" : "+v"(lq));
        const int eq = lq & 31, hq = lq >> 5;
        if (!first) {
            T4 = *(const u32x4*)(T + (size_t)c * NBLK + 4 * lq);
            __syncthreads();
            if (w * 64 + lq < 324) ldsst<uint32_t>(LB + L_HIST + (w * 64 + lq) * 4, 0u);
#pragma unroll
            for (int i = 0; i < 3; ++i) dma16(stg + 4096 + i * 1024, (uint32_t)((3 * w + i) * 1024 + lq * 16), WF);
        }
        bool more = false;
        uint32_t sum = 0;
#pragma unroll
        for (int i = 0; i < 4; ++i) {
            const int sg = 4 * lq + i;
            int cn = (int)(T4[i] >> 16) - 32 * round;
            if (sg >= nblk) cn = 0;
            more = more || cn > 32;
            cn = min(max(cn, 0), 32);
            if (sg < seg0) cn = 0;
            T4[i] = cn > 0 ? (((T4[i] & 0xFFFF) + 32 * round) | ((uint32_t)cn << 16)) : 0u;
            sum += cn;
        }
        u32x4 ra[4], rb[4];
        int cn4[4];
        const int L = w * 8 + (lq >> 3);
#pragma unroll
        for (int p = 0; p < 4; ++p) {
            const uint32_t tv = __shfl(T4[p], L, 64);
            cn4[p] = (int)(tv >> 16) - 4 * (lq & 7);
            const u32x2* rp = R + (4 * L + p) * CHUNK + (tv & 0xFFFF) + (cn4[p] > 0 ? 4 * (lq & 7) : 0);
            ra[p] = *(const u32x4*)rp; rb[p] = *(const u32x4*)(rp + 2);
        }
        const uint32_t xs = wave_scan(sum);
        uint32_t n = __builtin_amdgcn_readlane(xs, 63);
        int seg1 = NBLK;
        if (n > (uint32_t)CAP) {
            const int lcut = __popcll(__ballot(xs <= (uint32_t)CAP));
            n = __builtin_amdgcn_readlane(xs, lcut - 1);
            seg1 = 4 * lcut;
            if (L >= lcut) { cn4[0] = 0; cn4[1] = 0; cn4[2] = 0; cn4[3] = 0; }
        }
        const bool anymore = __any(more);
        const bool single = first && seg1 == NBLK && !anymore;
        T4 = u32x4{0u, 0u, 0u, 0u};
        asm volatile("s_waitcnt vmcnt(8) lgkmcnt(0)" ::: "memory");
        __builtin_amdgcn_s_barrier();
        STAMP(1, 2);
#define WFL(f) (LB + L_STG + ((f) / 3) * STG_W + 4096 + ((f) % 3) * 1024)
        if (first) {
            f32x16 p0, p1;
#pragma unroll
            for (int i = 0; i < 16; ++i) { p0[i] = 0.f; p1[i] = 0.f; }
#pragma unroll
            for (int s = 0; s < 4; ++s) {
                p0 = MFMA32(ldsld<bf16x8>(WFL(16 + s) + lq * 16), hb0[s], p0);
                p1 = MFMA32(ldsld<bf16x8>(WFL(20 + s) + lq * 16), hb0[s], p1);
            }
            const int nl = 32 * w + eq;
            if (nl < BINSZ) {
                const uint32_t pa = LB + L_P + nl * PROW + 16 * hq;
#pragma unroll
                for (int g = 0; g < 4; ++g) {
                    f32x4 a, b;
#pragma unroll
                    for (int i = 0; i < 4; ++i) { a[i] = p0[4 * g + i]; b[i] = p1[4 * g + i]; }
                    ldsst<f32x4>(pa + 32 * g, a); ldsst<f32x4>(pa + 128 + 32 * g, b);
                }
                if (hq == 0) { f32x4 pd0; pd0[0] = bf2f(pv0[0] & 0xFFFF); pd0[1] = bf2f(pv0[0] >> 16); pd0[2] = bf2f(pv0[1] & 0xFFFF); pd0[3] = 0.f; ldsst<f32x4>(pa + 256, pd0); }
            }
        }
#pragma unroll
        for (int s = 0; s < 4; ++s) hb0[s] = bf16x8{0, 0, 0, 0, 0, 0, 0, 0};
        pv0 = u32x2{0u, 0u};
        bf16x8 W2[8], Wa[2], Wx[4];
        s16x4 Wd[2];
#pragma unroll
        for (int i = 0; i < 8; ++i) W2[i] = ldsld<bf16x8>(WFL(i) + lq * 16);
#pragma unroll
        for (int i = 0; i < 2; ++i) { Wa[i] = ldsld<bf16x8>(WFL(8 + i) + lq * 16); Wd[i] = ldsld<s16x4>(WFL(10 + i) + lq * 16); }
#pragma unroll
        for (int i = 0; i < 4; ++i) Wx[i] = ldsld<bf16x8>(WFL(12 + i) + lq * 16);
        uint32_t ha[16], rk[16];
        const uint32_t dummy = 1024 + 4 * lq;
#pragma unroll
        for (int p = 0; p < 4; ++p) {
#pragma unroll
            for (int i = 0; i < 4; ++i) {
                const uint32_t w1v = i == 0 ? ra[p][1] : i == 1 ? ra[p][3] : i == 2 ? rb[p][1] : rb[p][3];
                ha[4 * p + i] = cn4[p] > i ? (w1v >> 16) : dummy;
                rk[4 * p + i] = __hip_atomic_fetch_add((lds_u32_p)(uintptr_t)(LB + L_HIST + ha[4 * p + i]), 1u, __ATOMIC_RELAXED, __HIP_MEMORY_SCOPE_WORKGROUP);
            }
        }
        STAMP(1, 3);
        __syncthreads();
        {
            const u32x4 hv = ldsld<u32x4>(LB + L_HIST + lq * 16);
            const uint32_t sm = hv[0] + hv[1] + hv[2] + hv[3], x = wave_scan(sm);
            u32x4 st; st[0] = x - sm; st[1] = st[0] + hv[0]; st[2] = st[1] + hv[1]; st[3] = st[2] + hv[2];
            ldsst<u32x4>(stg + lq * 16, st);
            ldsst<uint32_t>(stg + dummy, (uint32_t)TRASH);
            if (single) {
                const uint32_t t0 = (n * w) >> 3, t1 = (n * (w + 1)) >> 3;
                int a0 = 0, a1 = 0;
#pragma unroll
                for (int i = 0; i < 4; ++i) { a0 += __popcll(__ballot(st[i] < t0)); a1 += __popcll(__ballot(st[i] < t1)); }
                if (w == 7) a1 = BINSZ;
                a0 = min(a0, BINSZ); a1 = min(a1, BINSZ);
                if (a1 - a0 > 31) ldsst<uint32_t>(LB + L_FLAG, 1u);
                nb0 |= a0 << 16; nb1 |= a1 << 16;
            }
        }
        STAMP(1, 4);
#pragma unroll
        for (int k = 0; k < 16; ++k) rk[k] += ldsld<uint32_t>(stg + ha[k]);
#pragma unroll
        for (int p = 0; p < 4; ++p) {
#pragma unroll
            for (int i = 0; i < 4; ++i) {
                const uint32_t w0v = i == 0 ? ra[p][0] : i == 1 ? ra[p][2] : i == 2 ? rb[p][0] : rb[p][2];
                const uint32_t w1v = i == 0 ? ra[p][1] : i == 1 ? ra[p][3] : i == 2 ? rb[p][1] : rb[p][3];
                ldsst<uint16_t>(LB + L_SRC + rk[4 * p + i] * 2, (uint16_t)w1v);
                ldsst<uint32_t>(LB + L_W1 + rk[4 * p + i] * 4, w0v);
            }
        }
        STAMP(1, 5);
        asm volatile("" :: "v"(W2[0]), "v"(W2[1]), "v"(W2[2]), "v"(W2[3]), "v"(W2[4]), "v"(W2[5]), "v"(W2[6]), "v"(W2[7]));
        asm volatile("" :: "v"(Wa[0]), "v"(Wa[1]), "v"(Wd[0]), "v"(Wd[1]), "v"(Wx[0]), "v"(Wx[1]), "v"(Wx[2]), "v"(Wx[3]));
        __syncthreads();
        STAMP(1, 6);
        if (single) {
            const bool bad = __builtin_amdgcn_readfirstlane(ldsld<uint32_t>(LB + L_FLAG)) != 0;
            nb0 = bad ? (nb0 & 0xFFFF) : (nb0 >> 16); nb1 = bad ? (nb1 & 0xFFFF) : (nb1 >> 16);
        }
        first = false;
        const bool last = seg1 == NBLK && !anymore;
        if (seg1 == NBLK) { seg0 = 0; ++round; } else seg0 = seg1;
        const int nn = nb1 - nb0;
        const uint32_t lov = ldsld<uint32_t>(stg + (nb0 + min(eq, nn)) * 4);
        const uint32_t eb = __builtin_amdgcn_readfirstlane(lov);
        const uint32_t ee = __builtin_amdgcn_readlane(lov, 31);
        const int cntw = (int)(ee - eb);
        const int nt = (cntw + 31) >> 5;
        int lo = 16 * ((int)lov - (int)eb - 8 * hq);
        STAMP(1, 7);
        if (nt == 0) { if (last) break; continue; }

        uint32_t hfa[4];
#pragma unroll
        for (int s = 0; s < 4; ++s) hfa[s] = stg + eq * 128 + (((2 * s + hq) ^ ((eq >> 1) & 7)) << 4);
        const uint32_t lp0 = ((lq & 7) ^ ((lq >> 4) & 7)) << 4;
        const uint32_t lp1 = ((lq & 7) ^ ((4 + (lq >> 4)) & 7)) << 4;
        const uint32_t mbuf = stg + 8192;
        const uint32_t mwa = mbuf + eq * 72 + 8 * hq;
        const uint32_t tra = mbuf + (8 * hq + ((lq & 15) >> 2)) * 72 + ((lq >> 4) & 1) * 32 + (lq & 3) * 8;
        const uint32_t vwa = mbuf + lq * 8;
        const uint32_t vra = mbuf + (8 * hq + ((lq & 15) >> 2)) * 8;

        uint32_t w1A = 0, w1B = 0;
        uint32_t ix0, ix1, ix2, ix3, ixm;
        auto pf_idx = [&](int t, uint32_t& w1X) {
            const uint32_t li = eb + 32 * t, le = ee - 1;
            const uint32_t i8 = li + (lq >> 3);
            ix0 = ldsld<uint16_t>(LB + L_SRC + 2 * min(i8, le)); ix1 = ldsld<uint16_t>(LB + L_SRC + 2 * min(i8 + 8, le));
            ix2 = ldsld<uint16_t>(LB + L_SRC + 2 * min(i8 + 16, le)); ix3 = ldsld<uint16_t>(LB + L_SRC + 2 * min(i8 + 24, le));
            const uint32_t ie = min(li + eq, le);
            ixm = ldsld<uint16_t>(LB + L_SRC + 2 * ie);
            w1X = ldsld<uint32_t>(LB + L_W1 + 4 * ie);
        };
        auto pf_dma = [&](auto SET, int q, uint32_t ix) { dma16(stg + decltype(SET)::value * 4096 + q * 1024, (HM(ix) << 7) | ((q & 1) ? lp1 : lp0), hg); };
        auto pf_ea = [&](auto SET, uint32_t w1) {
            constexpr int AE = 240 + decltype(SET)::value * 4;
            const uint32_t eo = ((HE(w1) << 5) & 0x1FFFFFE0u) | (16 * hq);
            const char* bp = eag;
            asm volatile("global_load_dwordx4 v[%c2:%c3], %0, %1 " EAPOL :: "v"(eo), "s"(bp), "i"(AE), "i"(AE + 3) : "memory", ACLOB);
        };
        auto pf_pos = [&](auto SET) {
            constexpr int AP = 248 + decltype(SET)::value * 4;
            const char* bp = pos4g; const uint32_t po = HM(ixm) << 3;
            asm volatile("s_mov_b32 exec_hi, 0\n\tglobal_load_dwordx2 v[%c2:%c3], %0, %1\n\ts_mov_b32 exec_hi, -1" :: "v"(po), "s"(bp), "i"(AP), "i"(AP + 1) : "memory", ACLOB);
        };
        auto prefetch = [&](auto SET, int t, uint32_t& w1X) {
            pf_idx(t, w1X);
            pf_dma(SET, 0, ix0); pf_dma(SET, 1, ix1); pf_dma(SET, 2, ix2); pf_dma(SET, 3, ix3);
            pf_ea(SET, w1X); pf_pos(SET);
        };

        f32x16 x0, x1;
        bf16x8 hf[4];
        s16x4 d2v;
        float dxc, dyc, dzc;
        float dxp = 0.f, dyp = 0.f, dzp = 0.f;
        uint32_t pac;
        uint32_t m[8];
#pragma unroll
        for (int i = 0; i < 8; ++i) m[i] = 0u;
        f32x4 cs = {0.f, 0.f, 0.f, 0.f};
        u32x4 S0 = {0u, 0u, 0u, 0u}, S1 = {0u, 0u, 0u, 0u};
        bf16x8 A0, A1;
#pragma unroll
        for (int i = 0; i < 8; ++i) { A0[i] = 0; A1[i] = 0; }
        f32x4 pd;

        auto ldP = [&](f32x16& x, uint32_t a) {
#pragma unroll
            for (int g = 0; g < 4; ++g) {
                const f32x4 v = ldsld<f32x4>(a + 32 * g);
#pragma unroll
                for (int i = 0; i < 4; ++i) x[4 * g + i] = v[i];
            }
        };
        auto load_tile = [&](auto SET, uint32_t w1X) {
            const uint32_t prow = LB + L_P + (w1X >> 24) * PROW;
            pac = prow + 16 * hq;
            pd = ldsld<f32x4>(prow + 256);
            ldP(x0, pac);
#pragma unroll
            for (int s = 0; s < 4; ++s) hf[s] = ldsld<bf16x8>(hfa[s] + decltype(SET)::value * 4096);
        };
        auto mkdiff = [&](auto SET) {
            constexpr int AP = 248 + decltype(SET)::value * 4;
            const float p0 = pd[0], p1 = pd[1], p2 = pd[2]; float q0, q1, q2;
            asm volatile("v_lshlrev_b32 %0, 16, v%c3\n\tv_and_b32 %1, 0xffff0000, v%c3\n\tv_lshlrev_b32 %2, 16, v%c4"
                         : "=&v"(q0), "=&v"(q1), "=&v"(q2) : "i"(AP), "i"(AP + 1));
            dxc = p0 - q0; dyc = p1 - q1; dzc = p2 - q2;
            const float d2 = dxc * dxc + dyc * dyc + dzc * dzc;
            u32x2 dv; dv[0] = pkbf(d2, d2); dv[1] = dv[0];
            d2v = __builtin_bit_cast(s16x4, dv);
        };
        auto posupd = [&]() {
            const uint32_t c0 = __float_as_uint(cs[0]);
            const auto sw = __builtin_amdgcn_permlane32_swap(c0, c0, false, false);
            const float cv = __uint_as_float(sw[0]) + __uint_as_float(sw[1]);
            u32x2 vv; vv[0] = pkbf(dxp * cv, dyp * cv); vv[1] = pkbf(dzp * cv, dzp);
            ldsst<u32x2>(vwa, vv);
            const s16x4 a0 = trread(vra), a1 = trread(vra + 32), a2 = trread(vra + 128), a3 = trread(vra + 160);
            A0 = __builtin_shufflevector(a0, a1, 0, 1, 2, 3, 4, 5, 6, 7); A1 = __builtin_shufflevector(a2, a3, 0, 1, 2, 3, 4, 5, 6, 7);
            cs = f32x4{0.f, 0.f, 0.f, 0.f};
            dxp = dxc; dyp = dyc; dzp = dzc;
        };
        auto ea_mfma = [&](auto SET, f32x16& x, const bf16x8& wa) {
            constexpr int AE = 240 + decltype(SET)::value * 4;
            asm volatile("v_mfma_f32_32x32x16_bf16 %0, %1, v[%c2:%c3], %0" : "+v"(x) : "v"(wa), "i"(AE), "i"(AE + 3));
        };
        auto S_read = [&]() {
            const uint32_t ta0 = (uint32_t)min(max(lo, 0), 128), ta1 = (uint32_t)min(max(lo - 256, 0), 128);
            lo -= 512;
            S0 = ldsld<u32x4>(LB + L_TAB + ta0); S1 = ldsld<u32x4>(LB + L_TAB + ta1);
        };
        auto S_xor = [&]() {
#pragma unroll
            for (int i = 0; i < 4; ++i) {
                S0[i] ^= (uint32_t)__builtin_amdgcn_update_dpp(0, (int)S0[i], 0x130, 0xF, 0xF, false);
                S1[i] ^= (uint32_t)__builtin_amdgcn_update_dpp(0, (int)S1[i], 0x130, 0xF, 0xF, false);
            }
            asm volatile("" : "+v"(S0), "+v"(S1));
        };
        auto mwrite_tr = [&]() {
#pragma unroll
            for (int g = 0; g < 4; ++g) { u32x2 v; v[0] = m[2 * g]; v[1] = m[2 * g + 1]; ldsst<u32x2>(mwa + 16 * g, v); }
            const s16x4 a0 = trread(tra), a1 = trread(tra + 288), a2 = trread(tra + 1152), a3 = trread(tra + 1440);
            A0 = __builtin_shufflevector(a0, a1, 0, 1, 2, 3, 4, 5, 6, 7); A1 = __builtin_shufflevector(a2, a3, 0, 1, 2, 3, 4, 5, 6, 7);
        };
        auto csq = [&](int fb, int q) {
            const bf16x8& wv = Wx[2 * fb + (q >> 1)];
            const s16x4 a = (q & 1) ? __builtin_shufflevector(wv, wv, 4, 5, 6, 7) : __builtin_shufflevector(wv, wv, 0, 1, 2, 3);
            u32x2 b; b[0] = m[2 * q]; b[1] = m[2 * q + 1];
            cs = MFMA4(a, __builtin_bit_cast(s16x4, b), cs);
        };
#define SILU4(x, b, mo, SLOT1, SLOT2, SLOT3) do { \
            float t0 = x[4 * b] * NLOG2E, t1 = x[4 * b + 1] * NLOG2E, t2 = x[4 * b + 2] * NLOG2E, t3 = x[4 * b + 3] * NLOG2E; SB(); \
            SLOT1; SB(); \
            t0 = __builtin_amdgcn_exp2f(t0); t1 = __builtin_amdgcn_exp2f(t1); t2 = __builtin_amdgcn_exp2f(t2); t3 = __builtin_amdgcn_exp2f(t3); SB(); \
            SLOT2; SB(); \
            t0 += 1.f; t1 += 1.f; t2 += 1.f; t3 += 1.f; SB(); \
            t0 = __builtin_amdgcn_rcpf(t0); t1 = __builtin_amdgcn_rcpf(t1); t2 = __builtin_amdgcn_rcpf(t2); t3 = __builtin_amdgcn_rcpf(t3); SB(); \
            SLOT3; SB(); \
            t0 *= x[4 * b]; t1 *= x[4 * b + 1]; t2 *= x[4 * b + 2]; t3 *= x[4 * b + 3]; SB(); \
            mo[2 * b] = pkbf(t0, t1); mo[2 * b + 1] = pkbf(t2, t3); asm volatile("" : "+v"(mo[2 * b]), "+v"(mo[2 * b + 1])); SB(); } while (0)

        auto body = [&](auto SET, auto NXT, int t, uint32_t& w1X, uint32_t& w1N) {
            uint32_t mn[8];
            ldP(x1, pac + 128);
            pf_idx(t + 2, w1X);
            SB();
            SILU4(x0, 0, mn, (csq(1, 0), csq(1, 1)), (csq(1, 2), csq(1, 3)), Z1 = MFMA32(A0, BS(S0), Z1));
            SILU4(x0, 1, mn, Z1 = MFMA32(A1, BS(S1), Z1), ea_mfma(SET, x1, Wa[1]), (x1 = MFMA8(Wd[1], d2v, x1), posupd()));
            SILU4(x0, 2, mn, (x1 = MFMA32(W2[4], hf[0], x1), pf_dma(SET, 0, ix0)), (x1 = MFMA32(W2[5], hf[1], x1), pf_dma(SET, 1, ix1)), (x1 = MFMA32(W2[6], hf[2], x1), pf_dma(SET, 2, ix2)));
            SILU4(x0, 3, mn, (x1 = MFMA32(W2[7], hf[3], x1), pf_dma(SET, 3, ix3)), (Z2 = MFMA32(A0, BS(S0), Z2), pf_ea(SET, w1X)), (Z2 = MFMA32(A1, BS(S1), Z2), pf_pos(SET)));
#pragma unroll
            for (int i = 0; i < 8; ++i) m[i] = mn[i];
            mwrite_tr();
            S_read();
            asm volatile("s_waitcnt vmcnt(6)" ::: "memory");
            load_tile(NXT, w1N);
            SB();
            SILU4(x1, 0, mn, (csq(0, 0), csq(0, 1)), (csq(0, 2), csq(0, 3), S_xor()), Z0 = MFMA32(A0, BS(S0), Z0));
            SILU4(x1, 1, mn, (Z0 = MFMA32(A1, BS(S1), Z0), mkdiff(NXT)), ea_mfma(NXT, x0, Wa[0]), x0 = MFMA8(Wd[0], d2v, x0));
            SILU4(x1, 2, mn, x0 = MFMA32(W2[0], hf[0], x0), x0 = MFMA32(W2[1], hf[1], x0), x0 = MFMA32(W2[2], hf[2], x0));
            SILU4(x1, 3, mn, x0 = MFMA32(W2[3], hf[3], x0), (void)0, (void)0);
#pragma unroll
            for (int i = 0; i < 8; ++i) m[i] = mn[i];
            mwrite_tr();
            SB();
        };

        prefetch(IC<0>{}, 0, w1A);
        prefetch(IC<1>{}, 1, w1B);
        asm volatile("s_waitcnt vmcnt(6)" ::: "memory");
        load_tile(IC<0>{}, w1A);
        mkdiff(IC<0>{});
        ea_mfma(IC<0>{}, x0, Wa[0]);
        x0 = MFMA8(Wd[0], d2v, x0);
#pragma unroll
        for (int s = 0; s < 4; ++s) x0 = MFMA32(W2[s], hf[s], x0);
#pragma unroll 1
        for (int t = 0; t < nt; t += 2) {
            body(IC<0>{}, IC<1>{}, t, w1A, w1B);
            if (t + 1 < nt) body(IC<1>{}, IC<0>{}, t + 1, w1B, w1A);
        }
        asm volatile("s_waitcnt vmcnt(0)" ::: "memory");
        csq(1, 0); csq(1, 1); csq(1, 2); csq(1, 3);
        Z1 = MFMA32(A0, BS(S0), Z1);
        Z1 = MFMA32(A1, BS(S1), Z1);
        posupd();
        Z2 = MFMA32(A0, BS(S0), Z2);
        Z2 = MFMA32(A1, BS(S1), Z2);
        if (last) break;
    }

    STAMP(1, 8);
    {
        int lz = __builtin_amdgcn_mbcnt_hi(~0u, __builtin_amdgcn_mbcnt_lo(~0u, 0u)); asm volatile("" : "+v"(lz));
        const int ez = lz & 31, hz = lz >> 5;
        const int nn = nb1 - nb0;
        const int nl = nb0 + ez;
        const bool valid = ez < nn && nl < nnodes;
        const int ng = min(node0 + nl, N - 1);
        bf16x8 hb[4];
#pragma unroll
        for (int s = 0; s < 4; ++s) hb[s] = *(const bf16x8*)(hg + (size_t)ng * 64 + 16 * s + 8 * hz);
        u32x2 hv[8];
#pragma unroll
        for (int i = 0; i < 8; ++i) hv[i] = *(const u32x2*)(hg + (size_t)ng * 64 + 8 * i + 4 * hz);
        bf16x8 wf[16];
#pragma unroll
        for (int i = 0; i < 16; ++i) wf[i] = BS(WF[(24 + i) * 64 + lz]);
        u32x4 zb[4];
#pragma unroll
        for (int i = 0; i < 4; ++i) {
            zb[0][i] = pkbf(Z0[2 * i], Z0[2 * i + 1]); zb[1][i] = pkbf(Z0[8 + 2 * i], Z0[8 + 2 * i + 1]);
            zb[2][i] = pkbf(Z1[2 * i], Z1[2 * i + 1]); zb[3][i] = pkbf(Z1[8 + 2 * i], Z1[8 + 2 * i + 1]);
        }
#pragma unroll
        for (int ob = 0; ob < 2; ++ob) {
            f32x16 o;
#pragma unroll
            for (int i = 0; i < 16; ++i) o[i] = 0.f;
#pragma unroll
            for (int s = 0; s < 4; ++s) o = MFMA32(wf[ob * 4 + s], hb[s], o);
#pragma unroll
            for (int s = 0; s < 4; ++s) o = MFMA32(wf[8 + ob * 4 + s], BS(zb[s]), o);
            if (valid) {
#pragma unroll
                for (int g = 0; g < 4; ++g) {
                    const size_t off = (size_t)ng * 64 + 32 * ob + 8 * g + 4 * hz;
                    const u32x2 v = hv[4 * ob + g];
                    u32x2 r;
                    r[0] = pkbf(bf2f(v[0] & 0xFFFF) + silu1(o[4 * g]), bf2f(v[0] >> 16) + silu1(o[4 * g + 1]));
                    r[1] = pkbf(bf2f(v[1] & 0xFFFF) + silu1(o[4 * g + 2]), bf2f(v[1] >> 16) + silu1(o[4 * g + 3]));
                    *(u32x2*)(out + off) = r;
                }
            }
        }
        if (valid && hz == 0) {
            const uint32_t prow = LB + L_P + nl * PROW;
            const f32x4 pd = ldsld<f32x4>(prow + 256);
            uint16_t* po = out + (size_t)N * 64 + (size_t)ng * 3;
            po[0] = f2bf(pd[0] + Z2[0]); po[1] = f2bf(pd[1] + Z2[1]); po[2] = f2bf(pd[2] + Z2[2]);
        }
    }
    STAMP(1, 9);
    STAMPOUT(1);
    asm volatile(".Lcpf_z:");
}

static DI float rbf(float f) { return bf2f(f2bf(f)); }
__global__ void fb_zero(float* p, int n) { int i = blockIdx.x * 256 + threadIdx.x; if (i < n) p[i] = 0.f; }
__global__ void fb_edge(const uint16_t* h, const uint16_t* pos, const uint16_t* ea, const int* src, const int* dst,
                        const uint16_t* we, const uint16_t* wx, float* mi, float* pa, int E) {
    int e = blockIdx.x * 4 + (threadIdx.x >> 6);
    int f = threadIdx.x & 63;
    if (e >= E) return;
    size_t s = src[e], d = dst[e];
    float df0 = bf2f(pos[d * 3]) - bf2f(pos[s * 3]), df1 = bf2f(pos[d * 3 + 1]) - bf2f(pos[s * 3 + 1]), df2 = bf2f(pos[d * 3 + 2]) - bf2f(pos[s * 3 + 2]);
    float d2 = rbf(df0 * df0 + df1 * df1 + df2 * df2);
    float acc = 0.f;
    for (int k = 0; k < 64; ++k) acc += bf2f(h[d * 64 + k]) * bf2f(we[k * 64 + f]);
    for (int k = 0; k < 64; ++k) acc += bf2f(h[s * 64 + k]) * bf2f(we[(64 + k) * 64 + f]);
    acc += d2 * bf2f(we[128 * 64 + f]);
    for (int k = 0; k < 16; ++k) acc += bf2f(ea[(size_t)e * 16 + k]) * bf2f(we[(129 + k) * 64 + f]);
    float m = rbf(silu1(acc));
    float cs = m * bf2f(wx[f]);
    for (int o = 32; o > 0; o >>= 1) cs += __shfl_xor(cs, o, 64);
    atomicAdd(&mi[d * 64 + f], m);
    if (f < 3) atomicAdd(&pa[d * 3 + f], (f == 0 ? df0 : f == 1 ? df1 : df2) * cs);
}
__global__ void fb_node(const uint16_t* h, const uint16_t* pos, const uint16_t* wh, const float* mi, const float* pa, uint16_t* out, int N) {
    int n = blockIdx.x * 4 + (threadIdx.x >> 6);
    int f = threadIdx.x & 63;
    if (n >= N) return;
    float acc = 0.f;
    for (int k = 0; k < 64; ++k) acc += bf2f(h[(size_t)n * 64 + k]) * bf2f(wh[k * 64 + f]);
    for (int k = 0; k < 64; ++k) acc += rbf(mi[(size_t)n * 64 + k]) * bf2f(wh[(64 + k) * 64 + f]);
    out[(size_t)n * 64 + f] = f2bf(bf2f(h[(size_t)n * 64 + f]) + silu1(acc));
    if (f < 3) out[(size_t)N * 64 + (size_t)n * 3 + f] = f2bf(bf2f(pos[(size_t)n * 3 + f]) + pa[(size_t)n * 3 + f]);
}

extern "C" void kernel_launch(void* const* d_in, const int* in_sizes, int n_in,
                              void* d_out, int out_size, void* d_ws, size_t ws_size,
                              hipStream_t stream) {
    const uint16_t* h = (const uint16_t*)d_in[0];
    const uint16_t* pos = (const uint16_t*)d_in[1];
    const uint16_t* ea = (const uint16_t*)d_in[2];
    const int* src = (const int*)d_in[3];
    const int* dst = (const int*)d_in[4];
    const uint16_t* we = (const uint16_t*)d_in[5];
    const uint16_t* wx = (const uint16_t*)d_in[6];
    const uint16_t* wh = (const uint16_t*)d_in[7];
    const int N = in_sizes[0] / 64, E = in_sizes[3];
    const int nbin = (N + BINSZ - 1) / BINSZ, nblk = (E + CHUNK - 1) / CHUNK;
    if (nbin > NBIN || nblk > NBLK || N < 1 || E < 1) {
        float* mi = (float*)d_ws; float* pa = mi + (size_t)N * 64;
        int nz = N * 67;
        fb_zero<<<(nz + 255) / 256, 256, 0, stream>>>(mi, nz);
        if (E > 0) fb_edge<<<(E + 3) / 4, 256, 0, stream>>>(h, pos, ea, src, dst, we, wx, mi, pa, E);
        fb_node<<<(N + 3) / 4, 256, 0, stream>>>(h, pos, wh, mi, pa, (uint16_t*)d_out, N);
        return;
    }
    char* ws = (char*)d_ws;
    uint32_t* T = (uint32_t*)ws;
    u32x4* WF = (u32x4*)(ws + 262144);
    u32x2* pos4 = (u32x2*)(ws + 327680);
    u32x2* R = (u32x2*)(ws + 327680 + 802816);
    (void)hipFuncSetAttribute((const void*)k2_main, hipFuncAttributeMaxDynamicSharedMemorySize, L_TOTAL);
    k1_partition<<<nblk, 1024, 0, stream>>>(src, dst, pos, we, wx, wh, R, T, pos4, WF, N, E);
    k2_main<<<nbin, 512, L_TOTAL, stream>>>(h, (const char*)ea, (const char*)pos4, R, T, WF, (uint16_t*)d_out, N, nblk);
}
```

```cpp
#include <hip/hip_runtime.h>
#include <hip/hip_bf16.h>
#include <stdint.h>

using u32x4 = __attribute__((ext_vector_type(4))) unsigned;
using u32x2 = __attribute__((ext_vector_type(2))) unsigned;
using f32x16 = __attribute__((ext_vector_type(16))) float;
using f32x4 = __attribute__((ext_vector_type(4))) float;
using f32x2 = __attribute__((ext_vector_type(2))) float;
using bf16x8 = __attribute__((ext_vector_type(8))) short;
using s16x4 = __attribute__((ext_vector_type(4))) short;
using bf16x2 = __attribute__((ext_vector_type(2))) __bf16;
#define DI __device__ __forceinline__
#define ACLOB "v120", "v121", "v122", "v123", "v124", "v125", "v126", "v127"
#define MFMA16(a, b, c) __builtin_amdgcn_mfma_f32_16x16x32_bf16((a), (b), (c), 0, 0, 0)
#define MFMA32(a, b, c) __builtin_amdgcn_mfma_f32_32x32x16_bf16((a), (b), (c), 0, 0, 0)
#define MFMA8(a, b, c) __builtin_amdgcn_mfma_f32_32x32x8bf16_1k((a), (b), (c), 0, 0, 0)
#define MFMA4(a, b, c) __builtin_amdgcn_mfma_f32_4x4x4bf16_1k((a), (b), (c), 0, 0, 0)
#define SB() __builtin_amdgcn_sched_barrier(0)
#define BS(x) __builtin_bit_cast(bf16x8, x)

constexpr int CHUNK = 3136;
constexpr int BINSZ = 196;
constexpr int NBIN = 256;
constexpr int NBLK = 256;
constexpr int CAP = 3392;
constexpr int NPH = 8;
constexpr int TRASH = CAP + 160;
constexpr int LISTN = TRASH + 136;

constexpr int NW = 16;
constexpr int PROW = 272;
constexpr int L_WL = 0;
constexpr int L_HIST = 16 * 1024;
constexpr int L_DL = L_HIST + 320 * 4;
constexpr int L_SRC = L_DL + 4096;
constexpr int L_W1 = L_SRC + LISTN * 2;
constexpr int L_P = L_W1 + LISTN * 4;
constexpr int L_STG = ((L_P + BINSZ * PROW + 127) / 128) * 128;
constexpr int STG_W = 4096;
constexpr int L_TOTAL = L_STG + NW * STG_W;
static_assert(L_TOTAL <= 163840, "LDS");
static_assert(L_W1 % 16 == 0 && L_DL % 16 == 0 && L_HIST % 16 == 0 && L_P % 16 == 0 && LISTN <= 4096, "align");
constexpr float NLOG2E = -1.44269504089f;

DI float bf2f(uint32_t v) { return __uint_as_float(v << 16); }
DI uint32_t pkbf(float a, float b) { bf16x2 v; v[0] = (__bf16)a; v[1] = (__bf16)b; return __builtin_bit_cast(uint32_t, v); }
DI uint16_t f2bf(float a) { __bf16 v = (__bf16)a; return __builtin_bit_cast(uint16_t, v); }
DI float silu1(float x) { return x * __builtin_amdgcn_rcpf(1.f + __builtin_amdgcn_exp2f(x * NLOG2E)); }

DI uint32_t wave_scan(uint32_t x) {
    x += (uint32_t)__builtin_amdgcn_update_dpp(0, (int)x, 0x111, 0xF, 0xF, false);
    x += (uint32_t)__builtin_amdgcn_update_dpp(0, (int)x, 0x112, 0xF, 0xF, false);
    x += (uint32_t)__builtin_amdgcn_update_dpp(0, (int)x, 0x114, 0xF, 0xF, false);
    x += (uint32_t)__builtin_amdgcn_update_dpp(0, (int)x, 0x118, 0xF, 0xF, false);
    x += (uint32_t)__builtin_amdgcn_update_dpp(0, (int)x, 0x142, 0xA, 0xF, false);
    x += (uint32_t)__builtin_amdgcn_update_dpp(0, (int)x, 0x143, 0xC, 0xF, false);
    return x;
}

#define STAMPDECL
#define STAMP(K, i)
#define STAMPOUT(K)
#define EAPOL ""
#define HM(x) (x)
#define HE(x) (x)

DI const uint16_t* wf_src(int f, int l, int j, const uint16_t* we, const uint16_t* wx, const uint16_t* wh, bool& ok) {
    const int r = l & 31, h = l >> 5;
    const int kn = 8 * h + j, kp = 8 * (j >> 2) + 4 * h + (j & 3);
    ok = true;
    if (f < 8) return we + (64 + 16 * (f & 3) + kn) * 64 + 32 * (f >> 2) + r;
    if (f < 10) return we + (129 + kn) * 64 + 32 * (f - 8) + r;
    if (f < 12) { ok = kn == 0; return we + 128 * 64 + 32 * (f - 10) + r; }
    if (f < 16) return wx + 32 * ((f - 12) >> 1) + 16 * ((f - 12) & 1) + kp;
    if (f < 24) return we + (16 * ((f - 16) & 3) + kn) * 64 + 32 * ((f - 16) >> 2) + r;
    const int ob = (f - 24) >> 2, sx = (f - 24) & 3, kg = l >> 4, o = 16 * ob + (l & 15);
    if (sx < 2) return wh + (32 * sx + 8 * kg + j) * 64 + o;
    return wh + (64 + 32 * (sx - 2) + 16 * (j >> 2) + 4 * kg + (j & 3)) * 64 + o;
}

__global__ __launch_bounds__(1024) void k1_partition(const int* __restrict__ src, const int* __restrict__ dst,
                                                     const uint16_t* __restrict__ pos, const uint16_t* __restrict__ we, const uint16_t* __restrict__ wx,
                                                     const uint16_t* __restrict__ wh,
                                                     u32x2* __restrict__ R, uint32_t* __restrict__ T,
                                                     u32x2* __restrict__ pos4, u32x4* __restrict__ WF, int N, int E, uint32_t phmul) {
    __shared__ __attribute__((aligned(16))) uint32_t hist[NBIN];
    __shared__ __attribute__((aligned(16))) u32x2 recS[CHUNK];
    const int tid = threadIdx.x, blk = blockIdx.x;
    const int e0 = blk * CHUNK;
    const int cnt = min(CHUNK, E - e0);
    STAMPDECL;
    STAMP(0, 0);
    if (tid < NBIN) hist[tid] = 0;
    int d[4], s[4];
#pragma unroll
    for (int r = 0; r < 4; ++r) { int i = r * 1024 + tid; d[r] = i < cnt ? dst[e0 + i] : 0; s[r] = i < cnt ? src[e0 + i] : 0; }
    unsigned cpf = 0;
    {
        unsigned long long pc; unsigned sz;
        asm volatile("s_getpc_b64 %0\n.Lcpf_a:\n\ts_mov_b32 %1, .Lcpf_z-.Lcpf_a" : "=s"(pc), "=s"(sz));
        if (blk < 16 && (unsigned)tid * 64u + 4u <= sz) cpf = *(const __attribute__((address_space(1))) unsigned*)(pc + (unsigned)tid * 64u);
    }
    for (int f = blk; f < 40; f += gridDim.x) {
        if (tid < 512) {
            bool ok;
            const uint16_t* p = wf_src(f, tid >> 3, tid & 7, we, wx, wh, ok);
            const uint16_t v = *p;
            ((uint16_t*)WF)[f * 512 + tid] = ok ? v : (uint16_t)0;
        }
    }
    for (int n = blk * BINSZ + tid - 512; tid >= 512 && tid < 512 + BINSZ && n < N; n += gridDim.x * BINSZ) {
        u32x2 p; p[0] = pos[n * 3] | ((uint32_t)pos[n * 3 + 1] << 16); p[1] = pos[n * 3 + 2]; pos4[n] = p;
    }
    STAMP(0, 1);
    __syncthreads();
    uint32_t bin[4], rank[4];
#pragma unroll
    for (int r = 0; r < 4; ++r) {
        int i = r * 1024 + tid;
        bin[r] = (uint32_t)d[r] / BINSZ;
        rank[r] = 0;
        if (i < cnt) rank[r] = atomicAdd(&hist[bin[r]], 1u);
    }
    __syncthreads();
    STAMP(0, 2);
    if (tid < 64) {
        u32x4 hv = *(const u32x4*)(hist + 4 * tid);
        const uint32_t sum = hv[0] + hv[1] + hv[2] + hv[3], x = wave_scan(sum);
        u32x4 st; st[0] = x - sum; st[1] = st[0] + hv[0]; st[2] = st[1] + hv[1]; st[3] = st[2] + hv[2];
        *(u32x4*)(hist + 4 * tid) = st;
#pragma unroll
        for (int i = 0; i < 4; ++i) T[(4 * tid + i) * NBLK + blk] = st[i] | (hv[i] << 16);
    }
    __syncthreads();
    STAMP(0, 3);
#pragma unroll
    for (int r = 0; r < 4; ++r) {
        int i = r * 1024 + tid;
        if (i < cnt) {
            const uint32_t dl = d[r] - bin[r] * BINSZ;
            const uint32_t g5 = dl / 61, r5 = dl - 61 * g5;
            const uint32_t key = (5 * g5 + (r5 < 13 ? 0u : (r5 - 1) / 12)) * NPH + min(((uint32_t)s[r] * phmul) >> 20, (uint32_t)(NPH - 1));
            u32x2 rec; rec[0] = (uint32_t)(e0 + i) | (dl << 24); rec[1] = (uint32_t)s[r] | (key << 18);
            recS[hist[bin[r]] + rank[r]] = rec;
        }
    }
    __syncthreads();
    STAMP(0, 4);
#pragma unroll
    for (int r = 0; r < 2; ++r) {
        int i = r * 1024 + tid;
        if (2 * i < cnt) *(u32x4*)(R + e0 + 2 * i) = *(const u32x4*)(recS + 2 * i);
    }
    STAMP(0, 5);
    asm volatile("" :: "v"(cpf));
    STAMP(0, 6);
    STAMPOUT(0);
}

DI void dma16(uint32_t ldsaddr, uint32_t voff, const void* base) {
    asm volatile("s_mov_b32 m0, %0\n\ts_nop 0\n\tglobal_load_lds_dwordx4 %1, %2" :: "s"(ldsaddr), "v"(voff), "s"(base) : "memory");
}
typedef __attribute__((address_space(3))) s16x4* lds_s16x4_p;
typedef __attribute__((address_space(3))) uint32_t* lds_u32_p;
DI s16x4 trread(uint32_t addr) { return __builtin_amdgcn_ds_read_tr16_b64_v4i16((lds_s16x4_p)(uintptr_t)addr); }
template <class T> DI T ldsld(uint32_t addr) { return *(__attribute__((address_space(3))) T*)(uintptr_t)addr; }
template <class T> DI void ldsst(uint32_t addr, T v) { *(__attribute__((address_space(3))) T*)(uintptr_t)addr = v; }
template <int V> struct IC { static constexpr int value = V; };

__global__ __launch_bounds__(1024, 4) __attribute__((amdgpu_num_vgpr(60)))
void k2_main(const uint16_t* __restrict__ hg, const char* __restrict__ eag, const char* __restrict__ pos4g,
             const u32x2* __restrict__ R, const uint32_t* __restrict__ T,
             const u32x4* __restrict__ WF, uint16_t* __restrict__ out, int N, int nblk) {
    extern __shared__ __attribute__((aligned(16))) char ldsraw[];
    const uint32_t LB = (uint32_t)(uintptr_t)(__attribute__((address_space(3))) char*)ldsraw;
    const int tid = threadIdx.x, lane = tid & 63;
    const int w = __builtin_amdgcn_readfirstlane(tid >> 6);
    const int c = blockIdx.x;
    const int node0 = c * BINSZ;
    const int nnodes = min(BINSZ, N - node0);

    STAMPDECL;
    STAMP(1, 0);
    const uint32_t stg = LB + L_STG + w * STG_W;
    dma16(LB + L_WL + w * 1024, (uint32_t)(w * 1024 + lane * 16), WF);
    if (w < 8) dma16(stg + 2048, (uint32_t)((16 + w) * 1024 + lane * 16), WF);
    u32x4 T4 = *(const u32x4*)(T + (size_t)c * NBLK + 4 * lane);
    if (tid < 320) ldsst<uint32_t>(LB + L_HIST + tid * 4, 0u);
    ldsst<uint32_t>(LB + L_DL + tid * 4, 0xFFFFFFFFu);
    bf16x8 hb0[4];
    u32x2 pv0;
    {
        const int ng = min(node0 + 32 * min(w, 6) + (lane & 31), N - 1);
#pragma unroll
        for (int s = 0; s < 4; ++s) hb0[s] = *(const bf16x8*)(hg + (size_t)ng * 64 + 16 * s + 8 * (lane >> 5));
        pv0 = *(const u32x2*)(pos4g + (size_t)ng * 8);
    }
    STAMP(1, 1);
    f32x4 Z[4], Z2;
#pragma unroll
    for (int i = 0; i < 4; ++i) { Z[0][i] = 0.f; Z[1][i] = 0.f; Z[2][i] = 0.f; Z[3][i] = 0.f; Z2[i] = 0.f; }
    const int nb0 = 12 * w + (w + 4) / 5, nb1 = 12 * (w + 1) + (w + 5) / 5;
    asm volatile("v_mov_b32 v124, 0\n\tv_mov_b32 v125, 0" ::: ACLOB);

    int seg0 = 0, round = 0;
    bool first = true;
    for (;;) {
        int lq = __builtin_amdgcn_mbcnt_hi(~0u, __builtin_amdgcn_mbcnt_lo(~0u, 0u)); asm volatile("" : "+v"(lq));
        const int eq = lq & 31, hq = lq >> 5;
        if (!first) {
            T4 = *(const u32x4*)(T + (size_t)c * NBLK + 4 * lq);
            __syncthreads();
            if (w * 64 + lq < 320) ldsst<uint32_t>(LB + L_HIST + (w * 64 + lq) * 4, 0u);
            ldsst<uint32_t>(LB + L_DL + (w * 64 + lq) * 4, 0xFFFFFFFFu);
        }
        bool more = false;
        uint32_t sum = 0;
#pragma unroll
        for (int i = 0; i < 4; ++i) {
            const int sg = 4 * lq + i;
            int cn = (int)(T4[i] >> 16) - 32 * round;
            if (sg >= nblk) cn = 0;
            more = more || cn > 32;
            cn = min(max(cn, 0), 32);
            if (sg < seg0) cn = 0;
            T4[i] = cn > 0 ? (((T4[i] & 0xFFFF) + 32 * round) | ((uint32_t)cn << 16)) : 0u;
            sum += cn;
        }
        u32x4 ra[2], rb[2];
        int cn4[2];
        const int L = w * 4 + (lq >> 4), odd = (lq >> 3) & 1;
#pragma unroll
        for (int p = 0; p < 2; ++p) {
            const uint32_t t0 = __shfl(T4[p], L, 64), t1 = __shfl(T4[2 + p], L, 64);
            const uint32_t tv = odd ? t1 : t0;
            cn4[p] = (int)(tv >> 16) - 4 * (lq & 7);
            const u32x2* rp = R + (4 * L + 2 * odd + p) * CHUNK + (tv & 0xFFFF) + (cn4[p] > 0 ? 4 * (lq & 7) : 0);
            ra[p] = *(const u32x4*)rp; rb[p] = *(const u32x4*)(rp + 2);
        }
        const uint32_t xs = wave_scan(sum);
        uint32_t n = __builtin_amdgcn_readlane(xs, 63);
        int seg1 = NBLK;
        if (n > (uint32_t)CAP) {
            const int lcut = __popcll(__ballot(xs <= (uint32_t)CAP));
            n = __builtin_amdgcn_readlane(xs, lcut - 1);
            seg1 = 4 * lcut;
            if (L >= lcut) { cn4[0] = 0; cn4[1] = 0; }
        }
        const bool anymore = __any(more);
        T4 = u32x4{0u, 0u, 0u, 0u};
        asm volatile("s_waitcnt vmcnt(4) lgkmcnt(0)" ::: "memory");
        __builtin_amdgcn_s_barrier();
        STAMP(1, 2);
        if (first && w < 7) {
            f32x16 p0, p1;
#pragma unroll
            for (int i = 0; i < 16; ++i) { p0[i] = 0.f; p1[i] = 0.f; }
#pragma unroll
            for (int s = 0; s < 4; ++s) {
                p0 = MFMA32(ldsld<bf16x8>(LB + L_STG + s * STG_W + 2048 + lq * 16), hb0[s], p0);
                p1 = MFMA32(ldsld<bf16x8>(LB + L_STG + (4 + s) * STG_W + 2048 + lq * 16), hb0[s], p1);
            }
            const int nl = 32 * w + eq;
            if (nl < BINSZ) {
                const uint32_t pa = LB + L_P + nl * PROW + 16 * hq;
#pragma unroll
                for (int g = 0; g < 4; ++g) {
                    f32x4 a, b;
#pragma unroll
                    for (int i = 0; i < 4; ++i) { a[i] = p0[4 * g + i]; b[i] = p1[4 * g + i]; }
                    ldsst<f32x4>(pa + 32 * g, a); ldsst<f32x4>(pa + 128 + 32 * g, b);
                }
                if (hq == 0) { f32x4 pd0; pd0[0] = bf2f(pv0[0] & 0xFFFF); pd0[1] = bf2f(pv0[0] >> 16); pd0[2] = bf2f(pv0[1] & 0xFFFF); pd0[3] = 0.f; ldsst<f32x4>(pa + 256, pd0); }
            }
        }
#pragma unroll
        for (int s = 0; s < 4; ++s) hb0[s] = bf16x8{0, 0, 0, 0, 0, 0, 0, 0};
        pv0 = u32x2{0u, 0u};
        uint32_t ha[8], rk[8];
        const uint32_t dummy = 1024 + 4 * lq;
#pragma unroll
        for (int p = 0; p < 2; ++p) {
#pragma unroll
            for (int i = 0; i < 4; ++i) {
                const uint32_t w1v = i == 0 ? ra[p][1] : i == 1 ? ra[p][3] : i == 2 ? rb[p][1] : rb[p][3];
                ha[4 * p + i] = cn4[p] > i ? (w1v >> 16) : dummy;
                rk[4 * p + i] = __hip_atomic_fetch_add((lds_u32_p)(uintptr_t)(LB + L_HIST + ha[4 * p + i]), 1u, __ATOMIC_RELAXED, __HIP_MEMORY_SCOPE_WORKGROUP);
            }
        }
        STAMP(1, 3);
        __syncthreads();
        uint32_t eb, cntu;
        {
            const u32x4 hv = ldsld<u32x4>(LB + L_HIST + lq * 16);
            const uint32_t sm = hv[0] + hv[1] + hv[2] + hv[3];
            const uint32_t tw = sm + (uint32_t)__builtin_amdgcn_update_dpp(0, (int)sm, 0xB1, 0xF, 0xF, false);
            const uint32_t sp = sm + ((lq & 1) ? ((0u - tw) & 7u) : 0u);
            const uint32_t x = wave_scan(sp) - sp;
            u32x4 st; st[0] = x; st[1] = st[0] + hv[0]; st[2] = st[1] + hv[1]; st[3] = st[2] + hv[2];
            ldsst<u32x4>(stg + lq * 16, st);
            ldsst<uint32_t>(stg + dummy, (uint32_t)TRASH);
            eb = __builtin_amdgcn_readlane(x, 2 * w); cntu = __builtin_amdgcn_readlane(tw, 2 * w);
        }
        STAMP(1, 4);
#pragma unroll
        for (int k = 0; k < 8; ++k) rk[k] += ldsld<uint32_t>(stg + ha[k]);
#pragma unroll
        for (int p = 0; p < 2; ++p) {
#pragma unroll
            for (int i = 0; i < 4; ++i) {
                const uint32_t w0v = i == 0 ? ra[p][0] : i == 1 ? ra[p][2] : i == 2 ? rb[p][0] : rb[p][2];
                const uint32_t w1v = i == 0 ? ra[p][1] : i == 1 ? ra[p][3] : i == 2 ? rb[p][1] : rb[p][3];
                ldsst<uint16_t>(LB + L_SRC + rk[4 * p + i] * 2, (uint16_t)w1v);
                ldsst<uint32_t>(LB + L_W1 + rk[4 * p + i] * 4, w0v);
                ldsst<uint8_t>(LB + L_DL + rk[4 * p + i], (uint8_t)(w0v >> 24));
            }
        }
        STAMP(1, 5);
        __syncthreads();
        STAMP(1, 6);
        first = false;
        const bool last = seg1 == NBLK && !anymore;
        if (seg1 == NBLK) { seg0 = 0; ++round; } else seg0 = seg1;
        const int cq = lq & 15, kg = lq >> 4;
        const uint32_t ee = eb + cntu;
        const int nt = (int)(cntu + 31) >> 5;
        const uint32_t nrep = (uint32_t)(nb0 + cq) * 0x01010101u;
        uint32_t dla = LB + L_DL + eb + 8 * kg;
        STAMP(1, 7);
        if (nt == 0) { if (last) break; continue; }

        const uint32_t hfa = stg + eq * 128 + ((hq ^ ((eq >> 1) & 7)) << 4);
        const uint32_t lp0 = ((lq & 7) ^ ((lq >> 4) & 7)) << 4;
        const uint32_t mwa = stg + eq * 72 + 8 * hq;
        const uint32_t tra = stg + (8 * kg + (cq >> 2)) * 72 + (cq & 3) * 8;
        const uint32_t vwa = stg + 2304 + lq * 8;
        const uint32_t vra = stg + 2304 + (8 * kg + (cq >> 2)) * 8;
        const uint32_t wla = LB + L_WL + lq * 16;

        uint32_t w1c;
        auto pf_small = [&](int t) {
            const uint32_t ie = min(eb + 32 * t + eq, ee - 1);
            const uint32_t ixm = ldsld<uint16_t>(LB + L_SRC + 2 * ie);
            w1c = ldsld<uint32_t>(LB + L_W1 + 4 * ie);
            const uint32_t eo = ((HE(w1c) << 5) & 0x1FFFFFE0u) | (16 * hq), po = HM(ixm) << 3;
            const char* b0 = eag; const char* b1 = pos4g;
            asm volatile("global_load_dwordx4 v[120:123], %0, %2\n\ts_mov_b32 exec_hi, 0\n\tglobal_load_dwordx2 v[124:125], %1, %3\n\ts_mov_b32 exec_hi, -1"
                         :: "v"(eo), "v"(po), "s"(b0), "s"(b1) : "memory", ACLOB);
        };
        uint32_t ix0, ix1, ix2, ix3;
        auto pf_idx = [&](int t) {
            const uint32_t i8 = eb + 32 * t + (lq >> 3), le = ee - 1;
            ix0 = ldsld<uint16_t>(LB + L_SRC + 2 * min(i8, le)); ix1 = ldsld<uint16_t>(LB + L_SRC + 2 * min(i8 + 8, le));
            ix2 = ldsld<uint16_t>(LB + L_SRC + 2 * min(i8 + 16, le)); ix3 = ldsld<uint16_t>(LB + L_SRC + 2 * min(i8 + 24, le));
        };
        auto pf_dma = [&]() {
            dma16(stg, (HM(ix0) << 7) | lp0, hg); dma16(stg + 1024, (HM(ix1) << 7) | (lp0 ^ 64), hg);
            dma16(stg + 2048, (HM(ix2) << 7) | lp0, hg); dma16(stg + 3072, (HM(ix3) << 7) | (lp0 ^ 64), hg);
        };

        pf_idx(0);
        pf_dma();
        pf_small(0);
        const int th1 = (nt + 3) >> 2, th2 = (nt + 1) >> 1, th3 = (3 * nt + 3) >> 2;
        __builtin_amdgcn_s_setprio(3);
#pragma unroll 1
        for (int t = 0; t < nt; ++t) {
            if (t == th1) __builtin_amdgcn_s_setprio(2);
            if (t == th2) __builtin_amdgcn_s_setprio(1);
            if (t == th3) __builtin_amdgcn_s_setprio(0);
            asm volatile("s_waitcnt vmcnt(0)" ::: "memory");
            const uint32_t prow = LB + L_P + (w1c >> 24) * PROW;
            const uint32_t pac = prow + 16 * hq;
            const f32x4 pd = ldsld<f32x4>(prow + 256);
            bf16x8 hf[4];
#pragma unroll
            for (int s = 0; s < 4; ++s) hf[s] = ldsld<bf16x8>(hfa ^ (32 * s));
            float q0, q1, q2;
            asm volatile("v_lshlrev_b32 %0, 16, v124\n\tv_and_b32 %1, 0xffff0000, v124\n\tv_lshlrev_b32 %2, 16, v125" : "=&v"(q0), "=&v"(q1), "=&v"(q2));
            const float dx = pd[0] - q0, dy = pd[1] - q1, dz = pd[2] - q2;
            const float d2 = dx * dx + dy * dy + dz * dz;
            u32x2 dv; dv[0] = pkbf(d2, d2); dv[1] = dv[0];
            const s16x4 d2v = __builtin_bit_cast(s16x4, dv);
            u32x4 S;
            {
                const u32x2 dv8 = ldsld<u32x2>(dla);
                dla += 32;
#pragma unroll
                for (int i = 0; i < 2; ++i) {
                    const uint32_t tt = dv8[i] ^ nrep;
                    const uint32_t z = (~(((tt & 0x7F7F7F7Fu) + 0x7F7F7F7Fu) | tt) & 0x80808080u) >> 1;
                    S[2 * i] = __builtin_amdgcn_perm(0u, z, 0x010C000Cu); S[2 * i + 1] = __builtin_amdgcn_perm(0u, z, 0x030C020Cu);
                }
            }
            f32x4 cs = {0.f, 0.f, 0.f, 0.f};
#pragma unroll
            for (int fb = 0; fb < 2; ++fb) {
                f32x16 x;
#pragma unroll
                for (int g = 0; g < 4; ++g) {
                    const f32x4 v = ldsld<f32x4>(pac + 128 * fb + 32 * g);
#pragma unroll
                    for (int i = 0; i < 4; ++i) x[4 * g + i] = v[i];
                }
                { const bf16x8 wa = ldsld<bf16x8>(wla + (8 + fb) * 1024);
                  asm volatile("v_mfma_f32_32x32x16_bf16 %0, %1, v[120:123], %0" : "+v"(x) : "v"(wa)); }
                if (fb == 1) { if (t + 1 < nt) { pf_small(t + 1); pf_idx(t + 1); } }
                x = MFMA8(ldsld<s16x4>(wla + (10 + fb) * 1024), d2v, x);
#pragma unroll
                for (int s = 0; s < 4; ++s) x = MFMA32(ldsld<bf16x8>(wla + (4 * fb + s) * 1024), hf[s], x);
                uint32_t m[8];
#pragma unroll
                for (int b = 0; b < 4; ++b) {
                    float t0 = x[4 * b] * NLOG2E, t1 = x[4 * b + 1] * NLOG2E, t2 = x[4 * b + 2] * NLOG2E, t3 = x[4 * b + 3] * NLOG2E;
                    t0 = __builtin_amdgcn_exp2f(t0); t1 = __builtin_amdgcn_exp2f(t1); t2 = __builtin_amdgcn_exp2f(t2); t3 = __builtin_amdgcn_exp2f(t3);
                    t0 += 1.f; t1 += 1.f; t2 += 1.f; t3 += 1.f;
                    t0 = __builtin_amdgcn_rcpf(t0); t1 = __builtin_amdgcn_rcpf(t1); t2 = __builtin_amdgcn_rcpf(t2); t3 = __builtin_amdgcn_rcpf(t3);
                    m[2 * b] = pkbf(t0 * x[4 * b], t1 * x[4 * b + 1]); m[2 * b + 1] = pkbf(t2 * x[4 * b + 2], t3 * x[4 * b + 3]);
                }
#pragma unroll
                for (int g = 0; g < 4; ++g) { u32x2 v; v[0] = m[2 * g]; v[1] = m[2 * g + 1]; ldsst<u32x2>(mwa + 16 * g, v); }
#pragma unroll
                for (int q = 0; q < 2; ++q) {
                    const bf16x8 wv = ldsld<bf16x8>(wla + (12 + 2 * fb + q) * 1024);
                    u32x2 b0, b1; b0[0] = m[4 * q]; b0[1] = m[4 * q + 1]; b1[0] = m[4 * q + 2]; b1[1] = m[4 * q + 3];
                    cs = MFMA4(__builtin_shufflevector(wv, wv, 0, 1, 2, 3), __builtin_bit_cast(s16x4, b0), cs);
                    cs = MFMA4(__builtin_shufflevector(wv, wv, 4, 5, 6, 7), __builtin_bit_cast(s16x4, b1), cs);
                }
#pragma unroll
                for (int u = 0; u < 2; ++u) {
                    const s16x4 a0 = trread(tra + 32 * u), a1 = trread(tra + 32 * u + 288);
                    Z[2 * fb + u] = MFMA16(__builtin_shufflevector(a0, a1, 0, 1, 2, 3, 4, 5, 6, 7), BS(S), Z[2 * fb + u]);
                }
            }
            {
                const uint32_t c0 = __float_as_uint(cs[0]);
                const auto sw = __builtin_amdgcn_permlane32_swap(c0, c0, false, false);
                const float cv = __uint_as_float(sw[0]) + __uint_as_float(sw[1]);
                u32x2 vv; vv[0] = pkbf(dx * cv, dy * cv); vv[1] = pkbf(dz * cv, dz);
                ldsst<u32x2>(vwa, vv);
                const s16x4 a0 = trread(vra), a1 = trread(vra + 32);
                asm volatile("s_waitcnt lgkmcnt(0)" ::: "memory");
                if (t + 1 < nt) pf_dma();
                Z2 = MFMA16(__builtin_shufflevector(a0, a1, 0, 1, 2, 3, 4, 5, 6, 7), BS(S), Z2);
            }
        }
        __builtin_amdgcn_s_setprio(0);
        if (last) break;
    }

    STAMP(1, 8);
    {
        int lz = __builtin_amdgcn_mbcnt_hi(~0u, __builtin_amdgcn_mbcnt_lo(~0u, 0u)); asm volatile("" : "+v"(lz));
        const int cz = lz & 15, kz = lz >> 4;
        const int nn = nb1 - nb0;
        const int nl = nb0 + cz;
        const bool valid = cz < nn && nl < nnodes;
        const int ng = min(node0 + nl, N - 1);
        asm volatile("s_waitcnt lgkmcnt(0)" ::: "memory");
        dma16(stg, (uint32_t)((24 + w) * 1024 + lz * 16), WF);
        bf16x8 hb[2];
#pragma unroll
        for (int s = 0; s < 2; ++s) hb[s] = *(const bf16x8*)(hg + (size_t)ng * 64 + 32 * s + 8 * kz);
        u32x2 hv[4];
#pragma unroll
        for (int i = 0; i < 4; ++i) hv[i] = *(const u32x2*)(hg + (size_t)ng * 64 + 16 * i + 4 * kz);
        u32x4 zb[2];
#pragma unroll
        for (int s = 0; s < 2; ++s) {
            zb[s][0] = pkbf(Z[2 * s][0] * 0.5f, Z[2 * s][1] * 0.5f); zb[s][1] = pkbf(Z[2 * s][2] * 0.5f, Z[2 * s][3] * 0.5f);
            zb[s][2] = pkbf(Z[2 * s + 1][0] * 0.5f, Z[2 * s + 1][1] * 0.5f); zb[s][3] = pkbf(Z[2 * s + 1][2] * 0.5f, Z[2 * s + 1][3] * 0.5f);
        }
        asm volatile("s_waitcnt vmcnt(0)" ::: "memory");
        __builtin_amdgcn_s_barrier();
        const uint32_t wfa = LB + L_STG + lz * 16;
#pragma unroll
        for (int ob = 0; ob < 4; ++ob) {
            f32x4 o = {0.f, 0.f, 0.f, 0.f};
            o = MFMA16(ldsld<bf16x8>(wfa + (4 * ob) * STG_W), hb[0], o);
            o = MFMA16(ldsld<bf16x8>(wfa + (4 * ob + 1) * STG_W), hb[1], o);
            o = MFMA16(ldsld<bf16x8>(wfa + (4 * ob + 2) * STG_W), BS(zb[0]), o);
            o = MFMA16(ldsld<bf16x8>(wfa + (4 * ob + 3) * STG_W), BS(zb[1]), o);
            if (valid) {
                const u32x2 v = hv[ob];
                u32x2 r;
                r[0] = pkbf(bf2f(v[0] & 0xFFFF) + silu1(o[0]), bf2f(v[0] >> 16) + silu1(o[1]));
                r[1] = pkbf(bf2f(v[1] & 0xFFFF) + silu1(o[2]), bf2f(v[1] >> 16) + silu1(o[3]));
                *(u32x2*)(out + (size_t)ng * 64 + 16 * ob + 4 * kz) = r;
            }
        }
        if (valid && kz == 0) {
            const f32x4 pd = ldsld<f32x4>(LB + L_P + nl * PROW + 256);
            uint16_t* po = out + (size_t)N * 64 + (size_t)ng * 3;
            po[0] = f2bf(pd[0] + 0.5f * Z2[0]); po[1] = f2bf(pd[1] + 0.5f * Z2[1]); po[2] = f2bf(pd[2] + 0.5f * Z2[2]);
        }
    }
    STAMP(1, 9);
    STAMPOUT(1);
    asm volatile(".Lcpf_z:");
}

static DI float rbf(float f) { return bf2f(f2bf(f)); }
__global__ void fb_zero(float* p, int n) { int i = blockIdx.x * 256 + threadIdx.x; if (i < n) p[i] = 0.f; }
__global__ void fb_edge(const uint16_t* h, const uint16_t* pos, const uint16_t* ea, const int* src, const int* dst,
                        const uint16_t* we, const uint16_t* wx, float* mi, float* pa, int E) {
    int e = blockIdx.x * 4 + (threadIdx.x >> 6);
    int f = threadIdx.x & 63;
    if (e >= E) return;
    size_t s = src[e], d = dst[e];
    float df0 = bf2f(pos[d * 3]) - bf2f(pos[s * 3]), df1 = bf2f(pos[d * 3 + 1]) - bf2f(pos[s * 3 + 1]), df2 = bf2f(pos[d * 3 + 2]) - bf2f(pos[s * 3 + 2]);
    float d2 = rbf(df0 * df0 + df1 * df1 + df2 * df2);
    float acc = 0.f;
    for (int k = 0; k < 64; ++k) acc += bf2f(h[d * 64 + k]) * bf2f(we[k * 64 + f]);
    for (int k = 0; k < 64; ++k) acc += bf2f(h[s * 64 + k]) * bf2f(we[(64 + k) * 64 + f]);
    acc += d2 * bf2f(we[128 * 64 + f]);
    for (int k = 0; k < 16; ++k) acc += bf2f(ea[(size_t)e * 16 + k]) * bf2f(we[(129 + k) * 64 + f]);
    float m = rbf(silu1(acc));
    float cs = m * bf2f(wx[f]);
    for (int o = 32; o > 0; o >>= 1) cs += __shfl_xor(cs, o, 64);
    atomicAdd(&mi[d * 64 + f], m);
    if (f < 3) atomicAdd(&pa[d * 3 + f], (f == 0 ? df0 : f == 1 ? df1 : df2) * cs);
}
__global__ void fb_node(const uint16_t* h, const uint16_t* pos, const uint16_t* wh, const float* mi, const float* pa, uint16_t* out, int N) {
    int n = blockIdx.x * 4 + (threadIdx.x >> 6);
    int f = threadIdx.x & 63;
    if (n >= N) return;
    float acc = 0.f;
    for (int k = 0; k < 64; ++k) acc += bf2f(h[(size_t)n * 64 + k]) * bf2f(wh[k * 64 + f]);
    for (int k = 0; k < 64; ++k) acc += rbf(mi[(size_t)n * 64 + k]) * bf2f(wh[(64 + k) * 64 + f]);
    out[(size_t)n * 64 + f] = f2bf(bf2f(h[(size_t)n * 64 + f]) + silu1(acc));
    if (f < 3) out[(size_t)N * 64 + (size_t)n * 3 + f] = f2bf(bf2f(pos[(size_t)n * 3 + f]) + pa[(size_t)n * 3 + f]);
}

extern "C" void kernel_launch(void* const* d_in, const int* in_sizes, int n_in,
                              void* d_out, int out_size, void* d_ws, size_t ws_size,
                              hipStream_t stream) {
    const uint16_t* h = (const uint16_t*)d_in[0];
    const uint16_t* pos = (const uint16_t*)d_in[1];
    const uint16_t* ea = (const uint16_t*)d_in[2];
    const int* src = (const int*)d_in[3];
    const int* dst = (const int*)d_in[4];
    const uint16_t* we = (const uint16_t*)d_in[5];
    const uint16_t* wx = (const uint16_t*)d_in[6];
    const uint16_t* wh = (const uint16_t*)d_in[7];
    const int N = in_sizes[0] / 64, E = in_sizes[3];
    const int nbin = (N + BINSZ - 1) / BINSZ, nblk = (E + CHUNK - 1) / CHUNK;
    if (nbin > NBIN || nblk > NBLK || N < 1 || E < 1) {
        float* mi = (float*)d_ws; float* pa = mi + (size_t)N * 64;
        int nz = N * 67;
        fb_zero<<<(nz + 255) / 256, 256, 0, stream>>>(mi, nz);
        if (E > 0) fb_edge<<<(E + 3) / 4, 256, 0, stream>>>(h, pos, ea, src, dst, we, wx, mi, pa, E);
        fb_node<<<(N + 3) / 4, 256, 0, stream>>>(h, pos, wh, mi, pa, (uint16_t*)d_out, N);
        return;
    }
    char* ws = (char*)d_ws;
    uint32_t* T = (uint32_t*)ws;
    u32x4* WF = (u32x4*)(ws + 262144);
    u32x2* pos4 = (u32x2*)(ws + 327680);
    u32x2* R = (u32x2*)(ws + 327680 + 802816);
    (void)hipFuncSetAttribute((const void*)k2_main, hipFuncAttributeMaxDynamicSharedMemorySize, L_TOTAL);
    k1_partition<<<nblk, 1024, 0, stream>>>(src, dst, pos, we, wx, wh, R, T, pos4, WF, N, E, (uint32_t)((NPH << 20) / N));
    k2_main<<<nbin, 1024, L_TOTAL, stream>>>(h, (const char*)ea, (const char*)pos4, R, T, WF, (uint16_t*)d_out, N, nblk);
}
```

```cpp
#include <hip/hip_runtime.h>
#include <hip/hip_bf16.h>
#include <stdint.h>

using u32x4 = __attribute__((ext_vector_type(4))) unsigned;
using u32x2 = __attribute__((ext_vector_type(2))) unsigned;
using f32x16 = __attribute__((ext_vector_type(16))) float;
using f32x4 = __attribute__((ext_vector_type(4))) float;
using f32x2 = __attribute__((ext_vector_type(2))) float;
using bf16x8 = __attribute__((ext_vector_type(8))) short;
using s16x4 = __attribute__((ext_vector_type(4))) short;
using bf16x2 = __attribute__((ext_vector_type(2))) __bf16;
#define DI __device__ __forceinline__
#define ACLOB "v120", "v121", "v122", "v123", "v124", "v125", "v126", "v127"
#define MFMA16(a, b, c) __builtin_amdgcn_mfma_f32_16x16x32_bf16((a), (b), (c), 0, 0, 0)
#define MFMA32(a, b, c) __builtin_amdgcn_mfma_f32_32x32x16_bf16((a), (b), (c), 0, 0, 0)
#define MFMA8(a, b, c) __builtin_amdgcn_mfma_f32_32x32x8bf16_1k((a), (b), (c), 0, 0, 0)
#define MFMA4(a, b, c) __builtin_amdgcn_mfma_f32_4x4x4bf16_1k((a), (b), (c), 0, 0, 0)
#define SB() __builtin_amdgcn_sched_barrier(0)
#define BS(x) __builtin_bit_cast(bf16x8, x)

constexpr int CHUNK = 3136;
constexpr int BINSZ = 196;
constexpr int NBIN = 256;
constexpr int NBLK = 256;
constexpr int CAP = 3392;
#define NPHV 8
constexpr int NPH = NPHV;
constexpr int TRASH = CAP + 16 * 31 + 8;
constexpr int LISTN = TRASH + 136;

constexpr int NW = 16;
constexpr int PROW = 272;
constexpr int L_WL = 0;
constexpr int L_HIST = 12 * 1024;
constexpr int L_DL = L_HIST + 320 * 4;
constexpr int L_SRC = L_DL + 4096;
constexpr int L_W1 = L_SRC + LISTN * 2;
constexpr int L_P = L_W1 + LISTN * 4;
constexpr int L_STG = ((L_P + BINSZ * PROW + 127) / 128) * 128;
constexpr int STG_W = 4096;
constexpr int L_TOTAL = L_STG + NW * STG_W;
static_assert(L_TOTAL <= 163840, "LDS");
static_assert(L_W1 % 16 == 0 && L_DL % 16 == 0 && L_HIST % 16 == 0 && L_P % 16 == 0 && LISTN <= 4096 && (LISTN * 6) % 16 == 0, "align");
constexpr float NLOG2E = -1.44269504089f;

DI float bf2f(uint32_t v) { return __uint_as_float(v << 16); }
DI uint32_t pkbf(float a, float b) { bf16x2 v; v[0] = (__bf16)a; v[1] = (__bf16)b; return __builtin_bit_cast(uint32_t, v); }
DI uint16_t f2bf(float a) { __bf16 v = (__bf16)a; return __builtin_bit_cast(uint16_t, v); }
DI float silu1(float x) { return x * __builtin_amdgcn_rcpf(1.f + __builtin_amdgcn_exp2f(x * NLOG2E)); }

DI uint32_t wave_scan(uint32_t x) {
    x += (uint32_t)__builtin_amdgcn_update_dpp(0, (int)x, 0x111, 0xF, 0xF, false);
    x += (uint32_t)__builtin_amdgcn_update_dpp(0, (int)x, 0x112, 0xF, 0xF, false);
    x += (uint32_t)__builtin_amdgcn_update_dpp(0, (int)x, 0x114, 0xF, 0xF, false);
    x += (uint32_t)__builtin_amdgcn_update_dpp(0, (int)x, 0x118, 0xF, 0xF, false);
    x += (uint32_t)__builtin_amdgcn_update_dpp(0, (int)x, 0x142, 0xA, 0xF, false);
    x += (uint32_t)__builtin_amdgcn_update_dpp(0, (int)x, 0x143, 0xC, 0xF, false);
    return x;
}

#define STAMPDECL
#define STAMP(K, i)
#define STAMPOUT(K)
#define EAPOL ""
#define HM(x) (x)
#define HE(x) (x)

DI const uint16_t* wf_src(int f, int l, int j, const uint16_t* we, const uint16_t* wx, const uint16_t* wh, bool& ok) {
    const int r = l & 31, h = l >> 5;
    const int kn = 8 * h + j, kp = 8 * (j >> 2) + 4 * h + (j & 3);
    ok = true;
    if (f < 8) return we + (64 + 16 * (f & 3) + kn) * 64 + 32 * (f >> 2) + r;
    if (f < 10) return we + (129 + kn) * 64 + 32 * (f - 8) + r;
    if (f == 10) { ok = h == 0 && (j & 3) == 0; return we + 128 * 64 + 32 * (j >> 2) + r; }
    if (f == 11) { const int q = (l >> 1) & 3; ok = l < 8; return wx + 32 * (q >> 1) + 16 * (q & 1) + 8 * (j >> 2) + 4 * (l & 1) + (j & 3); }
    if (f < 16) { ok = false; return wx; }
    if (f < 24) return we + (16 * ((f - 16) & 3) + kn) * 64 + 32 * ((f - 16) >> 2) + r;
    const int ob = (f - 24) >> 2, sx = (f - 24) & 3, kg = l >> 4, o = 16 * ob + (l & 15);
    if (sx < 2) return wh + (32 * sx + 8 * kg + j) * 64 + o;
    return wh + (64 + 32 * (sx - 2) + 16 * (j >> 2) + 4 * kg + (j & 3)) * 64 + o;
}

__global__ __launch_bounds__(1024) void k1_partition(const int* __restrict__ src, const int* __restrict__ dst,
                                                     const uint16_t* __restrict__ pos, const uint16_t* __restrict__ we, const uint16_t* __restrict__ wx,
                                                     const uint16_t* __restrict__ wh,
                                                     u32x2* __restrict__ R, uint32_t* __restrict__ T,
                                                     u32x2* __restrict__ pos4, u32x4* __restrict__ WF, int N, int E, uint32_t phmul) {
    __shared__ __attribute__((aligned(16))) uint32_t hist[NBIN];
    __shared__ __attribute__((aligned(16))) u32x2 recS[CHUNK];
    const int tid = threadIdx.x, blk = blockIdx.x;
    const int e0 = blk * CHUNK;
    const int cnt = min(CHUNK, E - e0);
    STAMPDECL;
    STAMP(0, 0);
    if (tid < NBIN) hist[tid] = 0;
    int d[4], s[4];
#pragma unroll
    for (int r = 0; r < 4; ++r) { int i = r * 1024 + tid; d[r] = i < cnt ? dst[e0 + i] : 0; s[r] = i < cnt ? src[e0 + i] : 0; }
    unsigned cpf = 0;
    {
        unsigned long long pc; unsigned sz;
        asm volatile("s_getpc_b64 %0\n.Lcpf_a:\n\ts_mov_b32 %1, .Lcpf_z-.Lcpf_a" : "=s"(pc), "=s"(sz));
        if (blk < 16 && (unsigned)tid * 64u + 4u <= sz) cpf = *(const __attribute__((address_space(1))) unsigned*)(pc + (unsigned)tid * 64u);
    }
    for (int f = blk; f < 40; f += gridDim.x) {
        if (tid < 512) {
            bool ok;
            const uint16_t* p = wf_src(f, tid >> 3, tid & 7, we, wx, wh, ok);
            const uint16_t v = *p;
            ((uint16_t*)WF)[f * 512 + tid] = ok ? v : (uint16_t)0;
        }
    }
    for (int n = blk * BINSZ + tid - 512; tid >= 512 && tid < 512 + BINSZ && n < N; n += gridDim.x * BINSZ) {
        u32x2 p; p[0] = pos[n * 3] | ((uint32_t)pos[n * 3 + 1] << 16); p[1] = pos[n * 3 + 2]; pos4[n] = p;
    }
    STAMP(0, 1);
    __syncthreads();
    uint32_t bin[4], rank[4];
#pragma unroll
    for (int r = 0; r < 4; ++r) {
        int i = r * 1024 + tid;
        bin[r] = (uint32_t)d[r] / BINSZ;
        rank[r] = 0;
        if (i < cnt) rank[r] = atomicAdd(&hist[bin[r]], 1u);
    }
    __syncthreads();
    STAMP(0, 2);
    if (tid < 64) {
        u32x4 hv = *(const u32x4*)(hist + 4 * tid);
        const uint32_t sum = hv[0] + hv[1] + hv[2] + hv[3], x = wave_scan(sum);
        u32x4 st; st[0] = x - sum; st[1] = st[0] + hv[0]; st[2] = st[1] + hv[1]; st[3] = st[2] + hv[2];
        *(u32x4*)(hist + 4 * tid) = st;
#pragma unroll
        for (int i = 0; i < 4; ++i) T[(4 * tid + i) * NBLK + blk] = st[i] | (hv[i] << 16);
    }
    __syncthreads();
    STAMP(0, 3);
#pragma unroll
    for (int r = 0; r < 4; ++r) {
        int i = r * 1024 + tid;
        if (i < cnt) {
            const uint32_t dl = d[r] - bin[r] * BINSZ;
            const uint32_t g5 = dl / 61, r5 = dl - 61 * g5;
            const uint32_t key = (5 * g5 + (r5 < 13 ? 0u : (r5 - 1) / 12)) * NPH + min(((uint32_t)s[r] * phmul) >> 20, (uint32_t)(NPH - 1));
            u32x2 rec; rec[0] = (uint32_t)(e0 + i) | (dl << 24); rec[1] = (uint32_t)s[r] | (key << 18);
            recS[hist[bin[r]] + rank[r]] = rec;
        }
    }
    __syncthreads();
    STAMP(0, 4);
#pragma unroll
    for (int r = 0; r < 2; ++r) {
        int i = r * 1024 + tid;
        if (2 * i < cnt) *(u32x4*)(R + e0 + 2 * i) = *(const u32x4*)(recS + 2 * i);
    }
    STAMP(0, 5);
    asm volatile("" :: "v"(cpf));
    STAMP(0, 6);
    STAMPOUT(0);
}

DI void dma16(uint32_t ldsaddr, uint32_t voff, const void* base) {
    asm volatile("s_mov_b32 m0, %0\n\ts_nop 0\n\tglobal_load_lds_dwordx4 %1, %2" :: "s"(ldsaddr), "v"(voff), "s"(base) : "memory");
}
typedef __attribute__((address_space(3))) s16x4* lds_s16x4_p;
typedef __attribute__((address_space(3))) uint32_t* lds_u32_p;
DI s16x4 trread(uint32_t addr) { return __builtin_amdgcn_ds_read_tr16_b64_v4i16((lds_s16x4_p)(uintptr_t)addr); }
template <class T> DI T ldsld(uint32_t addr) { return *(__attribute__((address_space(3))) T*)(uintptr_t)addr; }
template <class T> DI void ldsst(uint32_t addr, T v) { *(__attribute__((address_space(3))) T*)(uintptr_t)addr = v; }
template <int V> struct IC { static constexpr int value = V; };

__global__ __launch_bounds__(1024, 4) __attribute__((amdgpu_num_vgpr(60)))
void k2_main(const uint16_t* __restrict__ hg, const char* __restrict__ eag, const char* __restrict__ pos4g,
             const u32x2* __restrict__ R, const uint32_t* __restrict__ T,
             const u32x4* __restrict__ WF, uint16_t* __restrict__ out, int N, int nblk) {
    extern __shared__ __attribute__((aligned(16))) char ldsraw[];
    const uint32_t LB = (uint32_t)(uintptr_t)(__attribute__((address_space(3))) char*)ldsraw;
    const int tid = threadIdx.x, lane = tid & 63;
    const int w = __builtin_amdgcn_readfirstlane(tid >> 6);
    const int c = blockIdx.x;
    const int node0 = c * BINSZ;
    const int nnodes = min(BINSZ, N - node0);

    STAMPDECL;
    STAMP(1, 0);
    const uint32_t stg = LB + L_STG + w * STG_W;
    if (w >= 4) dma16(LB + L_WL + (w - 4) * 1024, (uint32_t)((w - 4) * 1024 + lane * 16), WF);
    if (w < 8) dma16(stg + 2048, (uint32_t)((16 + w) * 1024 + lane * 16), WF);
    u32x4 T4 = *(const u32x4*)(T + (size_t)c * NBLK + 4 * lane);
    if (tid < 320) ldsst<uint32_t>(LB + L_HIST + tid * 4, 0u);
    ldsst<uint32_t>(LB + L_DL + tid * 4, 0xFFFFFFFFu);
    for (int i = tid; i < LISTN * 6 / 16; i += 1024) ldsst<u32x4>(LB + L_SRC + i * 16, u32x4{0u, 0u, 0u, 0u});
    if (w < 13) {
#pragma unroll
        for (int q = 0; q < 2; ++q) {
            const int row = 8 * q + (lane >> 3);
            const int ng = min(node0 + 16 * w + row, N - 1);
            dma16(stg + q * 1024, (uint32_t)(ng << 7) | (uint32_t)(((lane & 7) ^ ((row >> 1) & 7)) << 4), hg);
        }
    }
    if (tid < BINSZ) {
        const u32x2 pv = *(const u32x2*)(pos4g + (size_t)min(node0 + tid, N - 1) * 8);
        f32x4 pd0; pd0[0] = bf2f(pv[0] & 0xFFFF); pd0[1] = bf2f(pv[0] >> 16); pd0[2] = bf2f(pv[1] & 0xFFFF); pd0[3] = 0.f;
        ldsst<f32x4>(LB + L_P + tid * PROW + 256, pd0);
    }
    STAMP(1, 1);
    f32x4 Z[4], Z2;
#pragma unroll
    for (int i = 0; i < 4; ++i) { Z[0][i] = 0.f; Z[1][i] = 0.f; Z[2][i] = 0.f; Z[3][i] = 0.f; Z2[i] = 0.f; }
    const int nb0 = 12 * w + (w + 4) / 5, nb1 = 12 * (w + 1) + (w + 5) / 5;
    asm volatile("v_mov_b32 v124, 0\n\tv_mov_b32 v125, 0" ::: ACLOB);

    int seg0 = 0, round = 0;
    bool first = true, wdone = false;
    for (;;) {
        int lq = __builtin_amdgcn_mbcnt_hi(~0u, __builtin_amdgcn_mbcnt_lo(~0u, 0u)); asm volatile("" : "+v"(lq));
        const int eq = lq & 31, hq = lq >> 5;
        if (!first) {
            T4 = *(const u32x4*)(T + (size_t)c * NBLK + 4 * lq);
            __syncthreads();
            if (w * 64 + lq < 320) ldsst<uint32_t>(LB + L_HIST + (w * 64 + lq) * 4, 0u);
            ldsst<uint32_t>(LB + L_DL + (w * 64 + lq) * 4, 0xFFFFFFFFu);
            for (int i = w * 64 + lq; i < LISTN * 6 / 16; i += 1024) ldsst<u32x4>(LB + L_SRC + i * 16, u32x4{0u, 0u, 0u, 0u});
        }
        bool more = false;
        uint32_t sum = 0;
#pragma unroll
        for (int i = 0; i < 4; ++i) {
            const int sg = 4 * lq + i;
            int cn = (int)(T4[i] >> 16) - 32 * round;
            if (sg >= nblk) cn = 0;
            more = more || cn > 32;
            cn = min(max(cn, 0), 32);
            if (sg < seg0) cn = 0;
            T4[i] = cn > 0 ? (((T4[i] & 0xFFFF) + 32 * round) | ((uint32_t)cn << 16)) : 0u;
            sum += cn;
        }
        u32x4 ra[2], rb[2];
        int cn4[2];
        const int L = w * 4 + (lq >> 4), odd = (lq >> 3) & 1;
#pragma unroll
        for (int p = 0; p < 2; ++p) {
            const uint32_t t0 = __shfl(T4[p], L, 64), t1 = __shfl(T4[2 + p], L, 64);
            const uint32_t tv = odd ? t1 : t0;
            cn4[p] = (int)(tv >> 16) - 4 * (lq & 7);
            const u32x2* rp = R + (4 * L + 2 * odd + p) * CHUNK + (tv & 0xFFFF) + (cn4[p] > 0 ? 4 * (lq & 7) : 0);
            ra[p] = *(const u32x4*)rp; rb[p] = *(const u32x4*)(rp + 2);
        }
        const uint32_t xs = wave_scan(sum);
        uint32_t n = __builtin_amdgcn_readlane(xs, 63);
        int seg1 = NBLK;
        if (n > (uint32_t)CAP) {
            const int lcut = __popcll(__ballot(xs <= (uint32_t)CAP));
            n = __builtin_amdgcn_readlane(xs, lcut - 1);
            seg1 = 4 * lcut;
            if (L >= lcut) { cn4[0] = 0; cn4[1] = 0; }
        }
        const bool anymore = __any(more);
        T4 = u32x4{0u, 0u, 0u, 0u};
        asm volatile("s_waitcnt vmcnt(4) lgkmcnt(0)" ::: "memory");
        __builtin_amdgcn_s_barrier();
        STAMP(1, 2);
        if (first && w < 14) {
            f32x16 p0;
#pragma unroll
            for (int i = 0; i < 16; ++i) p0[i] = 0.f;
            const uint32_t wa = LB + L_STG + (w & 1) * 4 * STG_W + 2048 + lq * 16;
            const uint32_t ha0 = LB + L_STG + min((w & ~1) + (eq >> 4), 12) * STG_W + (eq & 15) * 128 + ((hq ^ ((eq >> 1) & 7)) << 4);
#pragma unroll
            for (int s = 0; s < 4; ++s) p0 = MFMA32(ldsld<bf16x8>(wa + s * STG_W), ldsld<bf16x8>(ha0 ^ (32 * s)), p0);
            const int nl = 32 * (w >> 1) + eq;
            if (nl < BINSZ) {
                const uint32_t pa = LB + L_P + nl * PROW + 16 * hq + 128 * (w & 1);
#pragma unroll
                for (int g = 0; g < 4; ++g) {
                    f32x4 a;
#pragma unroll
                    for (int i = 0; i < 4; ++i) a[i] = p0[4 * g + i];
                    ldsst<f32x4>(pa + 32 * g, a);
                }
            }
        }
        uint32_t ha[8], rk[8];
        const uint32_t dummy = 1024 + 4 * lq;
#pragma unroll
        for (int p = 0; p < 2; ++p) {
#pragma unroll
            for (int i = 0; i < 4; ++i) {
                const uint32_t w1v = i == 0 ? ra[p][1] : i == 1 ? ra[p][3] : i == 2 ? rb[p][1] : rb[p][3];
                ha[4 * p + i] = cn4[p] > i ? (w1v >> 16) : dummy;
                rk[4 * p + i] = __hip_atomic_fetch_add((lds_u32_p)(uintptr_t)(LB + L_HIST + ha[4 * p + i]), 1u, __ATOMIC_RELAXED, __HIP_MEMORY_SCOPE_WORKGROUP);
            }
        }
        STAMP(1, 3);
        __syncthreads();
        uint32_t eb, cntu;
        {
            const u32x4 hv = ldsld<u32x4>(LB + L_HIST + lq * 16);
            const uint32_t sm = hv[0] + hv[1] + hv[2] + hv[3];
            uint32_t tw = sm;
            if (NPH >= 8) tw += (uint32_t)__builtin_amdgcn_update_dpp(0, (int)tw, 0xB1, 0xF, 0xF, false);
            if (NPH >= 16) tw += (uint32_t)__builtin_amdgcn_update_dpp(0, (int)tw, 0x4E, 0xF, 0xF, false);
            const uint32_t sp = sm + ((lq & (NPH / 4 - 1)) == NPH / 4 - 1 ? ((0u - tw) & 31u) : 0u);
            const uint32_t x = wave_scan(sp) - sp;
            u32x4 st; st[0] = x; st[1] = st[0] + hv[0]; st[2] = st[1] + hv[1]; st[3] = st[2] + hv[2];
            ldsst<u32x4>(stg + lq * 16, st);
            ldsst<uint32_t>(stg + dummy, (uint32_t)TRASH);
            eb = __builtin_amdgcn_readlane(x, (NPH / 4) * w); cntu = __builtin_amdgcn_readlane(tw, (NPH / 4) * w);
        }
        STAMP(1, 4);
#pragma unroll
        for (int k = 0; k < 8; ++k) rk[k] += ldsld<uint32_t>(stg + ha[k]);
#pragma unroll
        for (int p = 0; p < 2; ++p) {
#pragma unroll
            for (int i = 0; i < 4; ++i) {
                const uint32_t w0v = i == 0 ? ra[p][0] : i == 1 ? ra[p][2] : i == 2 ? rb[p][0] : rb[p][2];
                const uint32_t w1v = i == 0 ? ra[p][1] : i == 1 ? ra[p][3] : i == 2 ? rb[p][1] : rb[p][3];
                ldsst<uint16_t>(LB + L_SRC + rk[4 * p + i] * 2, (uint16_t)w1v);
                ldsst<uint32_t>(LB + L_W1 + rk[4 * p + i] * 4, w0v);
                ldsst<uint8_t>(LB + L_DL + rk[4 * p + i], (uint8_t)(w0v >> 24));
            }
        }
        STAMP(1, 5);
        __syncthreads();
        STAMP(1, 6);
        first = false;
        const bool last = seg1 == NBLK && !anymore;
        if (seg1 == NBLK) { seg0 = 0; ++round; } else seg0 = seg1;
        const int cq = lq & 15, kg = lq >> 4;
        const int nt = (int)(cntu + 31) >> 5;
        const uint32_t nrep = (uint32_t)(nb0 + cq) * 0x01010101u;
        uint32_t dla = LB + L_DL + eb + 8 * kg;
        STAMP(1, 7);
        if (nt == 0) { if (last) break; continue; }

        const uint32_t hfa = stg + eq * 128 + ((hq ^ ((eq >> 1) & 7)) << 4);
        const uint32_t lp0 = ((lq & 7) ^ ((lq >> 4) & 7)) << 4, lp1 = lp0 ^ 64;
        const uint32_t mwa = stg + eq * 72 + 8 * hq;
        const uint32_t tra = stg + (8 * kg + (cq >> 2)) * 72 + (cq & 3) * 8;
        const uint32_t vwa = stg + 2304 + lq * 8;
        const uint32_t vra = stg + 2304 + (8 * kg + (cq >> 2)) * 8;
        const uint32_t wla = LB + L_WL + lq * 16, wxa = LB + L_WL + 11 * 1024 + hq * 16;

        uint32_t w1c;
        uint32_t cse = LB + L_SRC + 2 * (eb + eq), cw1 = LB + L_W1 + 4 * (eb + eq), cs8 = LB + L_SRC + 2 * (eb + (lq >> 3));
        auto pf_small = [&]() {
            const uint32_t ixm = ldsld<uint16_t>(cse);
            w1c = ldsld<uint32_t>(cw1);
            cse += 64; cw1 += 128;
            const uint32_t eo = ((HE(w1c) << 5) & 0x1FFFFFE0u) | (16 * hq), po = HM(ixm) << 3;
            const char* b0 = eag; const char* b1 = pos4g;
            asm volatile("global_load_dwordx4 v[120:123], %0, %2\n\ts_mov_b32 exec_hi, 0\n\tglobal_load_dwordx2 v[124:125], %1, %3\n\ts_mov_b32 exec_hi, -1"
                         :: "v"(eo), "v"(po), "s"(b0), "s"(b1) : "memory", ACLOB);
        };
        uint32_t ix0, ix1, ix2, ix3;
        auto pf_idx = [&]() {
            ix0 = ldsld<uint16_t>(cs8); ix1 = ldsld<uint16_t>(cs8 + 16); ix2 = ldsld<uint16_t>(cs8 + 32); ix3 = ldsld<uint16_t>(cs8 + 48);
            cs8 += 64;
        };
        auto pf_dma = [&]() {
            dma16(stg, (HM(ix0) << 7) | lp0, hg); dma16(stg + 1024, (HM(ix1) << 7) | lp1, hg);
            dma16(stg + 2048, (HM(ix2) << 7) | lp0, hg); dma16(stg + 3072, (HM(ix3) << 7) | lp1, hg);
        };

        pf_idx();
        pf_dma();
        pf_small();
        const int th1 = (nt + 3) >> 2, th2 = (nt + 1) >> 1, th3 = (3 * nt + 3) >> 2;
        __builtin_amdgcn_s_setprio(3);
#pragma unroll 1
        for (int t = 0; t < nt; ++t) {
            if (t == th1) __builtin_amdgcn_s_setprio(2);
            if (t == th2) __builtin_amdgcn_s_setprio(1);
            if (t == th3) __builtin_amdgcn_s_setprio(0);
            asm volatile("s_waitcnt vmcnt(0)" ::: "memory");
            const uint32_t prow = LB + L_P + (w1c >> 24) * PROW;
            const uint32_t pac = prow + 16 * hq;
            const f32x4 pd = ldsld<f32x4>(prow + 256);
            bf16x8 hf[4];
#pragma unroll
            for (int s = 0; s < 4; ++s) hf[s] = ldsld<bf16x8>(hfa ^ (32 * s));
            float q0, q1, q2;
            asm volatile("v_lshlrev_b32 %0, 16, v124\n\tv_and_b32 %1, 0xffff0000, v124\n\tv_lshlrev_b32 %2, 16, v125" : "=&v"(q0), "=&v"(q1), "=&v"(q2));
            const float dx = pd[0] - q0, dy = pd[1] - q1, dz = pd[2] - q2;
            const float d2 = dx * dx + dy * dy + dz * dz;
            u32x2 dv; dv[0] = pkbf(d2, d2); dv[1] = dv[0];
            const s16x4 d2v = __builtin_bit_cast(s16x4, dv);
            u32x4 S;
            {
                const u32x2 dv8 = ldsld<u32x2>(dla);
                dla += 32;
#pragma unroll
                for (int i = 0; i < 2; ++i) {
                    const uint32_t tt = dv8[i] ^ nrep;
                    const uint32_t z = (~(((tt & 0x7F7F7F7Fu) + 0x7F7F7F7Fu) | tt) & 0x80808080u) >> 1;
                    S[2 * i] = __builtin_amdgcn_perm(0u, z, 0x010C000Cu); S[2 * i + 1] = __builtin_amdgcn_perm(0u, z, 0x030C020Cu);
                }
            }
            f32x4 cs = {0.f, 0.f, 0.f, 0.f};
#pragma unroll
            for (int fb = 0; fb < 2; ++fb) {
                f32x16 x;
#pragma unroll
                for (int g = 0; g < 4; ++g) {
                    const f32x4 v = ldsld<f32x4>(pac + 128 * fb + 32 * g);
#pragma unroll
                    for (int i = 0; i < 4; ++i) x[4 * g + i] = v[i];
                }
                { const bf16x8 wa = ldsld<bf16x8>(wla + (8 + fb) * 1024);
                  asm volatile("v_mfma_f32_32x32x16_bf16 %0, %1, v[120:123], %0" : "+v"(x) : "v"(wa)); }
                if (fb == 1) { if (t + 1 < nt) { pf_small(); pf_idx(); } else if (last) { dma16(stg + 3072, (uint32_t)((24 + w) * 1024 + lq * 16), WF); wdone = true; } }
                x = MFMA8(ldsld<s16x4>(wla + 10 * 1024 + 8 * fb), d2v, x);
#pragma unroll
                for (int s = 0; s < 4; ++s) x = MFMA32(ldsld<bf16x8>(wla + (4 * fb + s) * 1024), hf[s], x);
                uint32_t m[8];
#pragma unroll
                for (int b = 0; b < 4; ++b) {
                    float t0 = x[4 * b] * NLOG2E, t1 = x[4 * b + 1] * NLOG2E, t2 = x[4 * b + 2] * NLOG2E, t3 = x[4 * b + 3] * NLOG2E;
                    t0 = __builtin_amdgcn_exp2f(t0); t1 = __builtin_amdgcn_exp2f(t1); t2 = __builtin_amdgcn_exp2f(t2); t3 = __builtin_amdgcn_exp2f(t3);
                    t0 += 1.f; t1 += 1.f; t2 += 1.f; t3 += 1.f;
                    t0 = __builtin_amdgcn_rcpf(t0); t1 = __builtin_amdgcn_rcpf(t1); t2 = __builtin_amdgcn_rcpf(t2); t3 = __builtin_amdgcn_rcpf(t3);
                    m[2 * b] = pkbf(t0 * x[4 * b], t1 * x[4 * b + 1]); m[2 * b + 1] = pkbf(t2 * x[4 * b + 2], t3 * x[4 * b + 3]);
                }
#pragma unroll
                for (int g = 0; g < 4; ++g) { u32x2 v; v[0] = m[2 * g]; v[1] = m[2 * g + 1]; ldsst<u32x2>(mwa + 16 * g, v); }
#pragma unroll
                for (int q = 0; q < 2; ++q) {
                    const bf16x8 wv = ldsld<bf16x8>(wxa + (2 * fb + q) * 32);
                    u32x2 b0, b1; b0[0] = m[4 * q]; b0[1] = m[4 * q + 1]; b1[0] = m[4 * q + 2]; b1[1] = m[4 * q + 3];
                    cs = MFMA4(__builtin_shufflevector(wv, wv, 0, 1, 2, 3), __builtin_bit_cast(s16x4, b0), cs);
                    cs = MFMA4(__builtin_shufflevector(wv, wv, 4, 5, 6, 7), __builtin_bit_cast(s16x4, b1), cs);
                }
#pragma unroll
                for (int u = 0; u < 2; ++u) {
                    const s16x4 a0 = trread(tra + 32 * u), a1 = trread(tra + 32 * u + 288);
                    Z[2 * fb + u] = MFMA16(__builtin_shufflevector(a0, a1, 0, 1, 2, 3, 4, 5, 6, 7), BS(S), Z[2 * fb + u]);
                }
            }
            {
                const uint32_t c0 = __float_as_uint(cs[0]);
                const auto sw = __builtin_amdgcn_permlane32_swap(c0, c0, false, false);
                const float cv = __uint_as_float(sw[0]) + __uint_as_float(sw[1]);
                u32x2 vv; vv[0] = pkbf(dx * cv, dy * cv); vv[1] = pkbf(dz * cv, dz);
                ldsst<u32x2>(vwa, vv);
                const s16x4 a0 = trread(vra), a1 = trread(vra + 32);
                asm volatile("s_waitcnt lgkmcnt(0)" ::: "memory");
                if (t + 1 < nt) pf_dma();
                Z2 = MFMA16(__builtin_shufflevector(a0, a1, 0, 1, 2, 3, 4, 5, 6, 7), BS(S), Z2);
            }
        }
        __builtin_amdgcn_s_setprio(0);
        if (last) break;
    }

    STAMP(1, 8);
    {
        int lz = __builtin_amdgcn_mbcnt_hi(~0u, __builtin_amdgcn_mbcnt_lo(~0u, 0u)); asm volatile("" : "+v"(lz));
        const int cz = lz & 15, kz = lz >> 4;
        const int nn = nb1 - nb0;
        const int nl = nb0 + cz;
        const bool valid = cz < nn && nl < nnodes;
        const int ng = min(node0 + nl, N - 1);
        asm volatile("s_waitcnt lgkmcnt(0)" ::: "memory");
        if (!wdone) dma16(stg + 3072, (uint32_t)((24 + w) * 1024 + lz * 16), WF);
#pragma unroll
        for (int q = 0; q < 2; ++q) {
            const int row = 8 * q + (lz >> 3);
            const int nr = min(node0 + nb0 + row, N - 1);
            dma16(stg + q * 1024, (uint32_t)(nr << 7) | (uint32_t)(((lz & 7) ^ ((row >> 1) & 7)) << 4), hg);
        }
        u32x4 zb[2];
#pragma unroll
        for (int s = 0; s < 2; ++s) {
            zb[s][0] = pkbf(Z[2 * s][0] * 0.5f, Z[2 * s][1] * 0.5f); zb[s][1] = pkbf(Z[2 * s][2] * 0.5f, Z[2 * s][3] * 0.5f);
            zb[s][2] = pkbf(Z[2 * s + 1][0] * 0.5f, Z[2 * s + 1][1] * 0.5f); zb[s][3] = pkbf(Z[2 * s + 1][2] * 0.5f, Z[2 * s + 1][3] * 0.5f);
        }
        asm volatile("s_waitcnt vmcnt(0)" ::: "memory");
        __builtin_amdgcn_s_barrier();
        const uint32_t wfa = LB + L_STG + 3072 + lz * 16;
        const uint32_t sw = (cz >> 1) & 7, rowa = stg + cz * 128;
        const bf16x8 hb0 = ldsld<bf16x8>(rowa + ((kz ^ sw) << 4)), hb1 = ldsld<bf16x8>(rowa + (((4 + kz) ^ sw) << 4));
#pragma unroll
        for (int ob = 0; ob < 4; ++ob) {
            f32x4 o = {0.f, 0.f, 0.f, 0.f};
            o = MFMA16(ldsld<bf16x8>(wfa + (4 * ob) * STG_W), hb0, o);
            o = MFMA16(ldsld<bf16x8>(wfa + (4 * ob + 1) * STG_W), hb1, o);
            o = MFMA16(ldsld<bf16x8>(wfa + (4 * ob + 2) * STG_W), BS(zb[0]), o);
            o = MFMA16(ldsld<bf16x8>(wfa + (4 * ob + 3) * STG_W), BS(zb[1]), o);
            const uint32_t pa = rowa + (((2 * ob + (kz >> 1)) ^ sw) << 4) + 8 * (kz & 1);
            const u32x2 v = ldsld<u32x2>(pa);
            u32x2 r;
            r[0] = pkbf(bf2f(v[0] & 0xFFFF) + silu1(o[0]), bf2f(v[0] >> 16) + silu1(o[1]));
            r[1] = pkbf(bf2f(v[1] & 0xFFFF) + silu1(o[2]), bf2f(v[1] >> 16) + silu1(o[3]));
            ldsst<u32x2>(pa, r);
        }
#pragma unroll
        for (int q = 0; q < 2; ++q) {
            const int row = 8 * q + (lz >> 3);
            const u32x4 v = ldsld<u32x4>(stg + q * 1024 + lz * 16);
            if (row < nn && nb0 + row < nnodes) *(u32x4*)(out + (size_t)(node0 + nb0 + row) * 64 + (((lz & 7) ^ ((row >> 1) & 7)) << 3)) = v;
        }
        if (valid && kz == 0) {
            const f32x4 pd = ldsld<f32x4>(LB + L_P + nl * PROW + 256);
            uint16_t* po = out + (size_t)N * 64 + (size_t)ng * 3;
            po[0] = f2bf(pd[0] + 0.5f * Z2[0]); po[1] = f2bf(pd[1] + 0.5f * Z2[1]); po[2] = f2bf(pd[2] + 0.5f * Z2[2]);
        }
    }
    STAMP(1, 9);
    STAMPOUT(1);
    asm volatile(".Lcpf_z:");
}

static DI float rbf(float f) { return bf2f(f2bf(f)); }
__global__ void fb_zero(float* p, int n) { int i = blockIdx.x * 256 + threadIdx.x; if (i < n) p[i] = 0.f; }
__global__ void fb_edge(const uint16_t* h, const uint16_t* pos, const uint16_t* ea, const int* src, const int* dst,
                        const uint16_t* we, const uint16_t* wx, float* mi, float* pa, int E) {
    int e = blockIdx.x * 4 + (threadIdx.x >> 6);
    int f = threadIdx.x & 63;
    if (e >= E) return;
    size_t s = src[e], d = dst[e];
    float df0 = bf2f(pos[d * 3]) - bf2f(pos[s * 3]), df1 = bf2f(pos[d * 3 + 1]) - bf2f(pos[s * 3 + 1]), df2 = bf2f(pos[d * 3 + 2]) - bf2f(pos[s * 3 + 2]);
    float d2 = rbf(df0 * df0 + df1 * df1 + df2 * df2);
    float acc = 0.f;
    for (int k = 0; k < 64; ++k) acc += bf2f(h[d * 64 + k]) * bf2f(we[k * 64 + f]);
    for (int k = 0; k < 64; ++k) acc += bf2f(h[s * 64 + k]) * bf2f(we[(64 + k) * 64 + f]);
    acc += d2 * bf2f(we[128 * 64 + f]);
    for (int k = 0; k < 16; ++k) acc += bf2f(ea[(size_t)e * 16 + k]) * bf2f(we[(129 + k) * 64 + f]);
    float m = rbf(silu1(acc));
    float cs = m * bf2f(wx[f]);
    for (int o = 32; o > 0; o >>= 1) cs += __shfl_xor(cs, o, 64);
    atomicAdd(&mi[d * 64 + f], m);
    if (f < 3) atomicAdd(&pa[d * 3 + f], (f == 0 ? df0 : f == 1 ? df1 : df2) * cs);
}
__global__ void fb_node(const uint16_t* h, const uint16_t* pos, const uint16_t* wh, const float* mi, const float* pa, uint16_t* out, int N) {
    int n = blockIdx.x * 4 + (threadIdx.x >> 6);
    int f = threadIdx.x & 63;
    if (n >= N) return;
    float acc = 0.f;
    for (int k = 0; k < 64; ++k) acc += bf2f(h[(size_t)n * 64 + k]) * bf2f(wh[k * 64 + f]);
    for (int k = 0; k < 64; ++k) acc += rbf(mi[(size_t)n * 64 + k]) * bf2f(wh[(64 + k) * 64 + f]);
    out[(size_t)n * 64 + f] = f2bf(bf2f(h[(size_t)n * 64 + f]) + silu1(acc));
    if (f < 3) out[(size_t)N * 64 + (size_t)n * 3 + f] = f2bf(bf2f(pos[(size_t)n * 3 + f]) + pa[(size_t)n * 3 + f]);
}

extern "C" void kernel_launch(void* const* d_in, const int* in_sizes, int n_in,
                              void* d_out, int out_size, void* d_ws, size_t ws_size,
                              hipStream_t stream) {
    const uint16_t* h = (const uint16_t*)d_in[0];
    const uint16_t* pos = (const uint16_t*)d_in[1];
    const uint16_t* ea = (const uint16_t*)d_in[2];
    const int* src = (const int*)d_in[3];
    const int* dst = (const int*)d_in[4];
    const uint16_t* we = (const uint16_t*)d_in[5];
    const uint16_t* wx = (const uint16_t*)d_in[6];
    const uint16_t* wh = (const uint16_t*)d_in[7];
    const int N = in_sizes[0] / 64, E = in_sizes[3];
    const int nbin = (N + BINSZ - 1) / BINSZ, nblk = (E + CHUNK - 1) / CHUNK;
    if (nbin > NBIN || nblk > NBLK || N < 1 || E < 1) {
        float* mi = (float*)d_ws; float* pa = mi + (size_t)N * 64;
        int nz = N * 67;
        fb_zero<<<(nz + 255) / 256, 256, 0, stream>>>(mi, nz);
        if (E > 0) fb_edge<<<(E + 3) / 4, 256, 0, stream>>>(h, pos, ea, src, dst, we, wx, mi, pa, E);
        fb_node<<<(N + 3) / 4, 256, 0, stream>>>(h, pos, wh, mi, pa, (uint16_t*)d_out, N);
        return;
    }
    char* ws = (char*)d_ws;
    uint32_t* T = (uint32_t*)ws;
    u32x4* WF = (u32x4*)(ws + 262144);
    u32x2* pos4 = (u32x2*)(ws + 327680);
    u32x2* R = (u32x2*)(ws + 327680 + 802816);
    (void)hipFuncSetAttribute((const void*)k2_main, hipFuncAttributeMaxDynamicSharedMemorySize, L_TOTAL);
    k1_partition<<<nblk, 1024, 0, stream>>>(src, dst, pos, we, wx, wh, R, T, pos4, WF, N, E, (uint32_t)((NPH << 20) / N));
    k2_main<<<nbin, 1024, L_TOTAL, stream>>>(h, (const char*)ea, (const char*)pos4, R, T, WF, (uint16_t*)d_out, N, nblk);
}
```

```cpp
#include <hip/hip_runtime.h>
#include <hip/hip_bf16.h>
#include <stdint.h>

using u32x4 = __attribute__((ext_vector_type(4))) unsigned;
using u32x2 = __attribute__((ext_vector_type(2))) unsigned;
using f32x16 = __attribute__((ext_vector_type(16))) float;
using f32x4 = __attribute__((ext_vector_type(4))) float;
using f32x2 = __attribute__((ext_vector_type(2))) float;
using bf16x8 = __attribute__((ext_vector_type(8))) short;
using s16x4 = __attribute__((ext_vector_type(4))) short;
using bf16x2 = __attribute__((ext_vector_type(2))) __bf16;
#define DI __device__ __forceinline__
#define ACLOB "v120", "v121", "v122", "v123", "v124", "v125", "v126", "v127"
#define MFMA16(a, b, c) __builtin_amdgcn_mfma_f32_16x16x32_bf16((a), (b), (c), 0, 0, 0)
#define MFMA32(a, b, c) __builtin_amdgcn_mfma_f32_32x32x16_bf16((a), (b), (c), 0, 0, 0)
#define MFMA8(a, b, c) __builtin_amdgcn_mfma_f32_32x32x8bf16_1k((a), (b), (c), 0, 0, 0)
#define MFMA4(a, b, c) __builtin_amdgcn_mfma_f32_4x4x4bf16_1k((a), (b), (c), 0, 0, 0)
#define SB() __builtin_amdgcn_sched_barrier(0)
#define BS(x) __builtin_bit_cast(bf16x8, x)

constexpr int CHUNK = 3136;
constexpr int BINSZ = 196;
constexpr int NBIN = 256;
constexpr int NBLK = 256;
constexpr int CAP = 3392;
constexpr int NPH = 8;
constexpr int TRASH = CAP + 16 * 31 + 8;
constexpr int LISTN = TRASH + 136;

constexpr int NW = 16;
constexpr int PROW = 272;
constexpr int L_WL = 0;
constexpr int L_HIST = 12 * 1024;
constexpr int L_DL = L_HIST + 320 * 4;
constexpr int L_SRC = L_DL + 4096;
constexpr int L_W1 = L_SRC + LISTN * 2;
constexpr int L_P = L_W1 + LISTN * 4;
constexpr int L_STG = ((L_P + BINSZ * PROW + 127) / 128) * 128;
constexpr int STG_W = 4096;
constexpr int L_TOTAL = L_STG + NW * STG_W;
static_assert(L_TOTAL <= 163840, "LDS");
static_assert(L_W1 % 16 == 0 && L_DL % 16 == 0 && L_HIST % 16 == 0 && L_P % 16 == 0 && LISTN <= 4096 && (LISTN * 6) % 16 == 0, "align");
constexpr float NLOG2E = -1.44269504089f;

DI float bf2f(uint32_t v) { return __uint_as_float(v << 16); }
DI uint32_t pkbf(float a, float b) { bf16x2 v; v[0] = (__bf16)a; v[1] = (__bf16)b; return __builtin_bit_cast(uint32_t, v); }
DI uint16_t f2bf(float a) { __bf16 v = (__bf16)a; return __builtin_bit_cast(uint16_t, v); }
DI float silu1(float x) { return x * __builtin_amdgcn_rcpf(1.f + __builtin_amdgcn_exp2f(x * NLOG2E)); }

DI uint32_t wave_scan(uint32_t x) {
    x += (uint32_t)__builtin_amdgcn_update_dpp(0, (int)x, 0x111, 0xF, 0xF, false);
    x += (uint32_t)__builtin_amdgcn_update_dpp(0, (int)x, 0x112, 0xF, 0xF, false);
    x += (uint32_t)__builtin_amdgcn_update_dpp(0, (int)x, 0x114, 0xF, 0xF, false);
    x += (uint32_t)__builtin_amdgcn_update_dpp(0, (int)x, 0x118, 0xF, 0xF, false);
    x += (uint32_t)__builtin_amdgcn_update_dpp(0, (int)x, 0x142, 0xA, 0xF, false);
    x += (uint32_t)__builtin_amdgcn_update_dpp(0, (int)x, 0x143, 0xC, 0xF, false);
    return x;
}

DI const uint16_t* wf_src(int f, int l, int j, const uint16_t* we, const uint16_t* wx, const uint16_t* wh, bool& ok) {
    const int r = l & 31, h = l >> 5;
    const int kn = 8 * h + j, kp = 8 * (j >> 2) + 4 * h + (j & 3);
    ok = true;
    if (f < 8) return we + (64 + 16 * (f & 3) + kn) * 64 + 32 * (f >> 2) + r;
    if (f < 10) return we + (129 + kn) * 64 + 32 * (f - 8) + r;
    if (f == 10) { ok = h == 0 && (j & 3) == 0; return we + 128 * 64 + 32 * (j >> 2) + r; }
    if (f == 11) { const int q = (l >> 1) & 3; ok = l < 8; return wx + 32 * (q >> 1) + 16 * (q & 1) + 8 * (j >> 2) + 4 * (l & 1) + (j & 3); }
    if (f < 16) { ok = false; return wx; }
    if (f < 24) return we + (16 * ((f - 16) & 3) + kn) * 64 + 32 * ((f - 16) >> 2) + r;
    const int ob = (f - 24) >> 2, sx = (f - 24) & 3, kg = l >> 4, o = 16 * ob + (l & 15);
    if (sx < 2) return wh + (32 * sx + 8 * kg + j) * 64 + o;
    return wh + (64 + 32 * (sx - 2) + 16 * (j >> 2) + 4 * kg + (j & 3)) * 64 + o;
}

__global__ __launch_bounds__(1024) void k1_partition(const int* __restrict__ src, const int* __restrict__ dst, const u32x4* __restrict__ h4,
                                                     const uint16_t* __restrict__ pos, const uint16_t* __restrict__ we, const uint16_t* __restrict__ wx,
                                                     const uint16_t* __restrict__ wh,
                                                     u32x2* __restrict__ R, uint32_t* __restrict__ T,
                                                     u32x2* __restrict__ pos4, u32x4* __restrict__ WF, int N, int E, uint32_t phmul) {
    __shared__ __attribute__((aligned(16))) uint32_t hist[NBIN];
    __shared__ __attribute__((aligned(16))) u32x2 recS[CHUNK];
    const int tid = threadIdx.x, blk = blockIdx.x;
    const int e0 = blk * CHUNK;
    const int cnt = min(CHUNK, E - e0);
    if (tid < NBIN) hist[tid] = 0;
    int d[4], s[4];
#pragma unroll
    for (int r = 0; r < 4; ++r) { int i = r * 1024 + tid; d[r] = i < cnt ? dst[e0 + i] : 0; s[r] = i < cnt ? src[e0 + i] : 0; }
    unsigned cpf = 0;
    {
        unsigned long long pc; unsigned sz;
        asm volatile("s_getpc_b64 %0\n.Lcpf_a:\n\ts_mov_b32 %1, .Lcpf_z-.Lcpf_a" : "=s"(pc), "=s"(sz));
        if (blk < 16 && sz < 65536u && (unsigned)tid * 64u + 4u <= sz) cpf = *(const __attribute__((address_space(1))) unsigned*)(pc + (unsigned)tid * 64u);
    }
    for (int f = blk; f < 40; f += gridDim.x) {
        if (tid < 512) {
            bool ok;
            const uint16_t* p = wf_src(f, tid >> 3, tid & 7, we, wx, wh, ok);
            const uint16_t v = *p;
            ((uint16_t*)WF)[f * 512 + tid] = ok ? v : (uint16_t)0;
        }
    }
    for (int n = blk * BINSZ + tid - 512; tid >= 512 && tid < 512 + BINSZ && n < N; n += gridDim.x * BINSZ) {
        u32x2 p; p[0] = pos[n * 3] | ((uint32_t)pos[n * 3 + 1] << 16); p[1] = pos[n * 3 + 2]; pos4[n] = p;
    }
    u32x4 wu[2];
#pragma unroll
    for (int r = 0; r < 2; ++r) { int i = blk * (BINSZ * 8) + r * 1024 + tid; wu[r] = h4[min(i, N * 8 - 1)]; }
    __syncthreads();
    uint32_t bin[4], rank[4];
#pragma unroll
    for (int r = 0; r < 4; ++r) {
        int i = r * 1024 + tid;
        bin[r] = (uint32_t)d[r] / BINSZ;
        rank[r] = 0;
        if (i < cnt) rank[r] = atomicAdd(&hist[bin[r]], 1u);
    }
    __syncthreads();
    if (tid < 64) {
        u32x4 hv = *(const u32x4*)(hist + 4 * tid);
        const uint32_t sum = hv[0] + hv[1] + hv[2] + hv[3], x = wave_scan(sum);
        u32x4 st; st[0] = x - sum; st[1] = st[0] + hv[0]; st[2] = st[1] + hv[1]; st[3] = st[2] + hv[2];
        *(u32x4*)(hist + 4 * tid) = st;
#pragma unroll
        for (int i = 0; i < 4; ++i) T[(4 * tid + i) * NBLK + blk] = st[i] | (hv[i] << 16);
    }
    __syncthreads();
#pragma unroll
    for (int r = 0; r < 4; ++r) {
        int i = r * 1024 + tid;
        if (i < cnt) {
            const uint32_t dl = d[r] - bin[r] * BINSZ;
            const uint32_t g5 = dl / 61, r5 = dl - 61 * g5;
            const uint32_t key = (5 * g5 + (r5 < 13 ? 0u : (r5 - 1) / 12)) * NPH + min(((uint32_t)s[r] * phmul) >> 20, (uint32_t)(NPH - 1));
            u32x2 rec; rec[0] = (uint32_t)(e0 + i) | (dl << 24); rec[1] = (uint32_t)s[r] | (key << 18);
            recS[hist[bin[r]] + rank[r]] = rec;
        }
    }
    __syncthreads();
#pragma unroll
    for (int r = 0; r < 2; ++r) {
        int i = r * 1024 + tid;
        if (2 * i < cnt) *(u32x4*)(R + e0 + 2 * i) = *(const u32x4*)(recS + 2 * i);
    }
    asm volatile("" :: "v"(cpf), "v"(wu[0]), "v"(wu[1]));
}

DI void dma16(uint32_t ldsaddr, uint32_t voff, const void* base) {
    asm volatile("s_mov_b32 m0, %0\n\ts_nop 0\n\tglobal_load_lds_dwordx4 %1, %2" :: "s"(ldsaddr), "v"(voff), "s"(base) : "memory");
}
typedef __attribute__((address_space(3))) s16x4* lds_s16x4_p;
typedef __attribute__((address_space(3))) uint32_t* lds_u32_p;
DI s16x4 trread(uint32_t addr) { return __builtin_amdgcn_ds_read_tr16_b64_v4i16((lds_s16x4_p)(uintptr_t)addr); }
template <class T> DI T ldsld(uint32_t addr) { return *(__attribute__((address_space(3))) T*)(uintptr_t)addr; }
template <class T> DI void ldsst(uint32_t addr, T v) { *(__attribute__((address_space(3))) T*)(uintptr_t)addr = v; }
template <int V> struct IC { static constexpr int value = V; };

__global__ __launch_bounds__(1024, 4) __attribute__((amdgpu_num_vgpr(60)))
void k2_main(const uint16_t* __restrict__ hg, const char* __restrict__ eag, const char* __restrict__ pos4g,
             const u32x2* __restrict__ R, const uint32_t* __restrict__ T,
             const u32x4* __restrict__ WF, uint16_t* __restrict__ out, int N, int nblk) {
    extern __shared__ __attribute__((aligned(16))) char ldsraw[];
    const uint32_t LB = (uint32_t)(uintptr_t)(__attribute__((address_space(3))) char*)ldsraw;
    const int tid = threadIdx.x, lane = tid & 63;
    const int w = __builtin_amdgcn_readfirstlane(tid >> 6);
    const int c = blockIdx.x;
    const int node0 = c * BINSZ;
    const int nnodes = min(BINSZ, N - node0);
    const uint32_t stg = LB + L_STG + w * STG_W;
    if (w >= 4) dma16(LB + L_WL + (w - 4) * 1024, (uint32_t)((w - 4) * 1024 + lane * 16), WF);
    if (w < 8) dma16(stg + 2048, (uint32_t)((16 + w) * 1024 + lane * 16), WF);
    u32x4 T4 = *(const u32x4*)(T + (size_t)c * NBLK + 4 * lane);
    if (tid < 320) ldsst<uint32_t>(LB + L_HIST + tid * 4, 0u);
    ldsst<uint32_t>(LB + L_DL + tid * 4, 0xFFFFFFFFu);
    for (int i = tid; i < LISTN * 6 / 16; i += 1024) ldsst<u32x4>(LB + L_SRC + i * 16, u32x4{0u, 0u, 0u, 0u});
    if (w < 13) {
#pragma unroll
        for (int q = 0; q < 2; ++q) {
            const int row = 8 * q + (lane >> 3);
            const int ng = min(node0 + 16 * w + row, N - 1);
            dma16(stg + q * 1024, (uint32_t)(ng << 7) | (uint32_t)(((lane & 7) ^ ((row >> 1) & 7)) << 4), hg);
        }
    }
    if (tid < BINSZ) {
        const u32x2 pv = *(const u32x2*)(pos4g + (size_t)min(node0 + tid, N - 1) * 8);
        f32x4 pd0; pd0[0] = bf2f(pv[0] & 0xFFFF); pd0[1] = bf2f(pv[0] >> 16); pd0[2] = bf2f(pv[1] & 0xFFFF); pd0[3] = 0.f;
        ldsst<f32x4>(LB + L_P + tid * PROW + 256, pd0);
    }
    f32x4 Z[4], Z2;
#pragma unroll
    for (int i = 0; i < 4; ++i) { Z[0][i] = 0.f; Z[1][i] = 0.f; Z[2][i] = 0.f; Z[3][i] = 0.f; Z2[i] = 0.f; }
    const int nb0 = 12 * w + (w + 4) / 5, nb1 = 12 * (w + 1) + (w + 5) / 5;
    asm volatile("v_mov_b32 v124, 0\n\tv_mov_b32 v125, 0" ::: ACLOB);

    int seg0 = 0, round = 0;
    bool first = true, wdone = false;
    for (;;) {
        int lq = __builtin_amdgcn_mbcnt_hi(~0u, __builtin_amdgcn_mbcnt_lo(~0u, 0u)); asm volatile("" : "+v"(lq));
        const int eq = lq & 31, hq = lq >> 5;
        if (!first) {
            T4 = *(const u32x4*)(T + (size_t)c * NBLK + 4 * lq);
            __syncthreads();
            if (w * 64 + lq < 320) ldsst<uint32_t>(LB + L_HIST + (w * 64 + lq) * 4, 0u);
            ldsst<uint32_t>(LB + L_DL + (w * 64 + lq) * 4, 0xFFFFFFFFu);
            for (int i = w * 64 + lq; i < LISTN * 6 / 16; i += 1024) ldsst<u32x4>(LB + L_SRC + i * 16, u32x4{0u, 0u, 0u, 0u});
        }
        bool more = false;
        uint32_t sum = 0;
#pragma unroll
        for (int i = 0; i < 4; ++i) {
            const int sg = 4 * lq + i;
            int cn = (int)(T4[i] >> 16) - 32 * round;
            if (sg >= nblk) cn = 0;
            more = more || cn > 32;
            cn = min(max(cn, 0), 32);
            if (sg < seg0) cn = 0;
            T4[i] = cn > 0 ? (((T4[i] & 0xFFFF) + 32 * round) | ((uint32_t)cn << 16)) : 0u;
            sum += cn;
        }
        u32x4 ra[2], rb[2];
        int cn4[2];
        const int L = w * 4 + (lq >> 4), odd = (lq >> 3) & 1;
#pragma unroll
        for (int p = 0; p < 2; ++p) {
            const uint32_t t0 = __shfl(T4[p], L, 64), t1 = __shfl(T4[2 + p], L, 64);
            const uint32_t tv = odd ? t1 : t0;
            cn4[p] = (int)(tv >> 16) - 4 * (lq & 7);
            const u32x2* rp = R + (4 * L + 2 * odd + p) * CHUNK + (tv & 0xFFFF) + (cn4[p] > 0 ? 4 * (lq & 7) : 0);
            ra[p] = *(const u32x4*)rp; rb[p] = *(const u32x4*)(rp + 2);
        }
        const uint32_t xs = wave_scan(sum);
        uint32_t n = __builtin_amdgcn_readlane(xs, 63);
        int seg1 = NBLK;
        if (n > (uint32_t)CAP) {
            const int lcut = __popcll(__ballot(xs <= (uint32_t)CAP));
            n = __builtin_amdgcn_readlane(xs, lcut - 1);
            seg1 = 4 * lcut;
            if (L >= lcut) { cn4[0] = 0; cn4[1] = 0; }
        }
        const bool anymore = __any(more);
        T4 = u32x4{0u, 0u, 0u, 0u};
        asm volatile("s_waitcnt vmcnt(4) lgkmcnt(0)" ::: "memory");
        __builtin_amdgcn_s_barrier();
        if (first && w < 14) {
            f32x16 p0;
#pragma unroll
            for (int i = 0; i < 16; ++i) p0[i] = 0.f;
            const uint32_t wa = LB + L_STG + (w & 1) * 4 * STG_W + 2048 + lq * 16;
            const uint32_t ha0 = LB + L_STG + min((w & ~1) + (eq >> 4), 12) * STG_W + (eq & 15) * 128 + ((hq ^ ((eq >> 1) & 7)) << 4);
#pragma unroll
            for (int s = 0; s < 4; ++s) p0 = MFMA32(ldsld<bf16x8>(wa + s * STG_W), ldsld<bf16x8>(ha0 ^ (32 * s)), p0);
            const int nl = 32 * (w >> 1) + eq;
            if (nl < BINSZ) {
                const uint32_t pa = LB + L_P + nl * PROW + 16 * hq + 128 * (w & 1);
#pragma unroll
                for (int g = 0; g < 4; ++g) {
                    f32x4 a;
#pragma unroll
                    for (int i = 0; i < 4; ++i) a[i] = p0[4 * g + i];
                    ldsst<f32x4>(pa + 32 * g, a);
                }
            }
        }
        uint32_t ha[8], rk[8];
        const uint32_t dummy = 1024 + 4 * lq;
#pragma unroll
        for (int p = 0; p < 2; ++p) {
#pragma unroll
            for (int i = 0; i < 4; ++i) {
                const uint32_t w1v = i == 0 ? ra[p][1] : i == 1 ? ra[p][3] : i == 2 ? rb[p][1] : rb[p][3];
                ha[4 * p + i] = cn4[p] > i ? (w1v >> 16) : dummy;
                rk[4 * p + i] = __hip_atomic_fetch_add((lds_u32_p)(uintptr_t)(LB + L_HIST + ha[4 * p + i]), 1u, __ATOMIC_RELAXED, __HIP_MEMORY_SCOPE_WORKGROUP);
            }
        }
        __syncthreads();
        uint32_t eb, cntu;
        {
            const u32x4 hv = ldsld<u32x4>(LB + L_HIST + lq * 16);
            const uint32_t sm = hv[0] + hv[1] + hv[2] + hv[3];
            uint32_t tw = sm;
            if (NPH >= 8) tw += (uint32_t)__builtin_amdgcn_update_dpp(0, (int)tw, 0xB1, 0xF, 0xF, false);
            if (NPH >= 16) tw += (uint32_t)__builtin_amdgcn_update_dpp(0, (int)tw, 0x4E, 0xF, 0xF, false);
            const uint32_t sp = sm + ((lq & (NPH / 4 - 1)) == NPH / 4 - 1 ? ((0u - tw) & 31u) : 0u);
            const uint32_t x = wave_scan(sp) - sp;
            u32x4 st; st[0] = x; st[1] = st[0] + hv[0]; st[2] = st[1] + hv[1]; st[3] = st[2] + hv[2];
            ldsst<u32x4>(stg + lq * 16, st);
            ldsst<uint32_t>(stg + dummy, (uint32_t)TRASH);
            eb = __builtin_amdgcn_readlane(x, (NPH / 4) * w); cntu = __builtin_amdgcn_readlane(tw, (NPH / 4) * w);
        }
#pragma unroll
        for (int k = 0; k < 8; ++k) rk[k] += ldsld<uint32_t>(stg + ha[k]);
#pragma unroll
        for (int p = 0; p < 2; ++p) {
#pragma unroll
            for (int i = 0; i < 4; ++i) {
                const uint32_t w0v = i == 0 ? ra[p][0] : i == 1 ? ra[p][2] : i == 2 ? rb[p][0] : rb[p][2];
                const uint32_t w1v = i == 0 ? ra[p][1] : i == 1 ? ra[p][3] : i == 2 ? rb[p][1] : rb[p][3];
                ldsst<uint16_t>(LB + L_SRC + rk[4 * p + i] * 2, (uint16_t)w1v);
                ldsst<uint32_t>(LB + L_W1 + rk[4 * p + i] * 4, w0v);
                ldsst<uint8_t>(LB + L_DL + rk[4 * p + i], (uint8_t)(w0v >> 24));
            }
        }
        __syncthreads();
        first = false;
        const bool last = seg1 == NBLK && !anymore;
        if (seg1 == NBLK) { seg0 = 0; ++round; } else seg0 = seg1;
        const int cq = lq & 15, kg = lq >> 4;
        const int nt = (int)(cntu + 31) >> 5;
        const uint32_t nrep = (uint32_t)(nb0 + cq) * 0x01010101u;
        uint32_t dla = LB + L_DL + eb + 8 * kg;
        if (nt == 0) { if (last) break; continue; }

        const uint32_t hfa = stg + eq * 128 + ((hq ^ ((eq >> 1) & 7)) << 4);
        const uint32_t lp0 = ((lq & 7) ^ ((lq >> 4) & 7)) << 4, lp1 = lp0 ^ 64;
        const uint32_t mwa = stg + eq * 72 + 8 * hq;
        const uint32_t tra = stg + (8 * kg + (cq >> 2)) * 72 + (cq & 3) * 8;
        const uint32_t vwa = stg + 2304 + lq * 8;
        const uint32_t vra = stg + 2304 + (8 * kg + (cq >> 2)) * 8;
        const uint32_t wla = LB + L_WL + lq * 16, wxa = LB + L_WL + 11 * 1024 + hq * 16;

        uint32_t w1c;
        uint32_t cse = LB + L_SRC + 2 * (eb + eq), cw1 = LB + L_W1 + 4 * (eb + eq), cs8 = LB + L_SRC + 2 * (eb + (lq >> 3));
        auto pf_small = [&]() {
            const uint32_t ixm = ldsld<uint16_t>(cse);
            w1c = ldsld<uint32_t>(cw1);
            cse += 64; cw1 += 128;
            const uint32_t eo = ((w1c << 5) & 0x1FFFFFE0u) | (16 * hq), po = ixm << 3;
            const char* b0 = eag; const char* b1 = pos4g;
            asm volatile("global_load_dwordx4 v[120:123], %0, %2\n\ts_mov_b32 exec_hi, 0\n\tglobal_load_dwordx2 v[124:125], %1, %3\n\ts_mov_b32 exec_hi, -1"
                         :: "v"(eo), "v"(po), "s"(b0), "s"(b1) : "memory", ACLOB);
        };
        uint32_t ix0, ix1, ix2, ix3;
        auto pf_idx = [&]() {
            ix0 = ldsld<uint16_t>(cs8); ix1 = ldsld<uint16_t>(cs8 + 16); ix2 = ldsld<uint16_t>(cs8 + 32); ix3 = ldsld<uint16_t>(cs8 + 48);
            cs8 += 64;
        };
        auto pf_dma = [&]() {
            dma16(stg, (ix0 << 7) | lp0, hg); dma16(stg + 1024, (ix1 << 7) | lp1, hg);
            dma16(stg + 2048, (ix2 << 7) | lp0, hg); dma16(stg + 3072, (ix3 << 7) | lp1, hg);
        };

        pf_idx();
        pf_dma();
        pf_small();
        const int th1 = (nt + 3) >> 2, th2 = (nt + 1) >> 1, th3 = (3 * nt + 3) >> 2;
        __builtin_amdgcn_s_setprio(3);
#pragma unroll 1
        for (int t = 0; t < nt; ++t) {
            if (t == th1) __builtin_amdgcn_s_setprio(2);
            if (t == th2) __builtin_amdgcn_s_setprio(1);
            if (t == th3) __builtin_amdgcn_s_setprio(0);
            asm volatile("s_waitcnt vmcnt(0)" ::: "memory");
            const uint32_t prow = LB + L_P + (w1c >> 24) * PROW;
            const uint32_t pac = prow + 16 * hq;
            const f32x4 pd = ldsld<f32x4>(prow + 256);
            bf16x8 hf[4];
#pragma unroll
            for (int s = 0; s < 4; ++s) hf[s] = ldsld<bf16x8>(hfa ^ (32 * s));
            float q0, q1, q2;
            asm volatile("v_lshlrev_b32 %0, 16, v124\n\tv_and_b32 %1, 0xffff0000, v124\n\tv_lshlrev_b32 %2, 16, v125" : "=&v"(q0), "=&v"(q1), "=&v"(q2));
            const float dx = pd[0] - q0, dy = pd[1] - q1, dz = pd[2] - q2;
            const float d2 = dx * dx + dy * dy + dz * dz;
            u32x2 dv; dv[0] = pkbf(d2, d2); dv[1] = dv[0];
            const s16x4 d2v = __builtin_bit_cast(s16x4, dv);
            u32x4 S;
            {
                const u32x2 dv8 = ldsld<u32x2>(dla);
                dla += 32;
#pragma unroll
                for (int i = 0; i < 2; ++i) {
                    const uint32_t tt = dv8[i] ^ nrep;
                    const uint32_t z = (~(((tt & 0x7F7F7F7Fu) + 0x7F7F7F7Fu) | tt) & 0x80808080u) >> 1;
                    S[2 * i] = __builtin_amdgcn_perm(0u, z, 0x010C000Cu); S[2 * i + 1] = __builtin_amdgcn_perm(0u, z, 0x030C020Cu);
                }
            }
            f32x4 cs = {0.f, 0.f, 0.f, 0.f};
#pragma unroll
            for (int fb = 0; fb < 2; ++fb) {
                f32x16 x;
#pragma unroll
                for (int g = 0; g < 4; ++g) {
                    const f32x4 v = ldsld<f32x4>(pac + 128 * fb + 32 * g);
#pragma unroll
                    for (int i = 0; i < 4; ++i) x[4 * g + i] = v[i];
                }
                { const bf16x8 wa = ldsld<bf16x8>(wla + (8 + fb) * 1024);
                  asm volatile("v_mfma_f32_32x32x16_bf16 %0, %1, v[120:123], %0" : "+v"(x) : "v"(wa)); }
                if (fb == 1) { if (t + 1 < nt) { pf_small(); pf_idx(); } else if (last) { dma16(stg + 3072, (uint32_t)((24 + w) * 1024 + lq * 16), WF); wdone = true; } }
                x = MFMA8(ldsld<s16x4>(wla + 10 * 1024 + 8 * fb), d2v, x);
#pragma unroll
                for (int s = 0; s < 4; ++s) x = MFMA32(ldsld<bf16x8>(wla + (4 * fb + s) * 1024), hf[s], x);
                uint32_t m[8];
#pragma unroll
                for (int b = 0; b < 4; ++b) {
                    float t0 = x[4 * b] * NLOG2E, t1 = x[4 * b + 1] * NLOG2E, t2 = x[4 * b + 2] * NLOG2E, t3 = x[4 * b + 3] * NLOG2E;
                    t0 = __builtin_amdgcn_exp2f(t0); t1 = __builtin_amdgcn_exp2f(t1); t2 = __builtin_amdgcn_exp2f(t2); t3 = __builtin_amdgcn_exp2f(t3);
                    t0 += 1.f; t1 += 1.f; t2 += 1.f; t3 += 1.f;
                    t0 = __builtin_amdgcn_rcpf(t0); t1 = __builtin_amdgcn_rcpf(t1); t2 = __builtin_amdgcn_rcpf(t2); t3 = __builtin_amdgcn_rcpf(t3);
                    m[2 * b] = pkbf(t0 * x[4 * b], t1 * x[4 * b + 1]); m[2 * b + 1] = pkbf(t2 * x[4 * b + 2], t3 * x[4 * b + 3]);
                }
#pragma unroll
                for (int g = 0; g < 4; ++g) { u32x2 v; v[0] = m[2 * g]; v[1] = m[2 * g + 1]; ldsst<u32x2>(mwa + 16 * g, v); }
#pragma unroll
                for (int q = 0; q < 2; ++q) {
                    const bf16x8 wv = ldsld<bf16x8>(wxa + (2 * fb + q) * 32);
                    u32x2 b0, b1; b0[0] = m[4 * q]; b0[1] = m[4 * q + 1]; b1[0] = m[4 * q + 2]; b1[1] = m[4 * q + 3];
                    cs = MFMA4(__builtin_shufflevector(wv, wv, 0, 1, 2, 3), __builtin_bit_cast(s16x4, b0), cs);
                    cs = MFMA4(__builtin_shufflevector(wv, wv, 4, 5, 6, 7), __builtin_bit_cast(s16x4, b1), cs);
                }
#pragma unroll
                for (int u = 0; u < 2; ++u) {
                    const s16x4 a0 = trread(tra + 32 * u), a1 = trread(tra + 32 * u + 288);
                    Z[2 * fb + u] = MFMA16(__builtin_shufflevector(a0, a1, 0, 1, 2, 3, 4, 5, 6, 7), BS(S), Z[2 * fb + u]);
                }
            }
            {
                const uint32_t c0 = __float_as_uint(cs[0]);
                const auto sw = __builtin_amdgcn_permlane32_swap(c0, c0, false, false);
                const float cv = __uint_as_float(sw[0]) + __uint_as_float(sw[1]);
                u32x2 vv; vv[0] = pkbf(dx * cv, dy * cv); vv[1] = pkbf(dz * cv, dz);
                ldsst<u32x2>(vwa, vv);
                const s16x4 a0 = trread(vra), a1 = trread(vra + 32);
                asm volatile("s_waitcnt lgkmcnt(0)" ::: "memory");
                if (t + 1 < nt) pf_dma();
                Z2 = MFMA16(__builtin_shufflevector(a0, a1, 0, 1, 2, 3, 4, 5, 6, 7), BS(S), Z2);
            }
        }
        __builtin_amdgcn_s_setprio(0);
        if (last) break;
    }
    {
        int lz = __builtin_amdgcn_mbcnt_hi(~0u, __builtin_amdgcn_mbcnt_lo(~0u, 0u)); asm volatile("" : "+v"(lz));
        const int cz = lz & 15, kz = lz >> 4;
        const int nn = nb1 - nb0;
        const int nl = nb0 + cz;
        const bool valid = cz < nn && nl < nnodes;
        const int ng = min(node0 + nl, N - 1);
        asm volatile("s_waitcnt lgkmcnt(0)" ::: "memory");
        if (!wdone) dma16(stg + 3072, (uint32_t)((24 + w) * 1024 + lz * 16), WF);
#pragma unroll
        for (int q = 0; q < 2; ++q) {
            const int row = 8 * q + (lz >> 3);
            const int nr = min(node0 + nb0 + row, N - 1);
            dma16(stg + q * 1024, (uint32_t)(nr << 7) | (uint32_t)(((lz & 7) ^ ((row >> 1) & 7)) << 4), hg);
        }
        u32x4 zb[2];
#pragma unroll
        for (int s = 0; s < 2; ++s) {
            zb[s][0] = pkbf(Z[2 * s][0] * 0.5f, Z[2 * s][1] * 0.5f); zb[s][1] = pkbf(Z[2 * s][2] * 0.5f, Z[2 * s][3] * 0.5f);
            zb[s][2] = pkbf(Z[2 * s + 1][0] * 0.5f, Z[2 * s + 1][1] * 0.5f); zb[s][3] = pkbf(Z[2 * s + 1][2] * 0.5f, Z[2 * s + 1][3] * 0.5f);
        }
        asm volatile("s_waitcnt vmcnt(0)" ::: "memory");
        __builtin_amdgcn_s_barrier();
        const uint32_t wfa = LB + L_STG + 3072 + lz * 16;
        const uint32_t sw = (cz >> 1) & 7, rowa = stg + cz * 128;
        const bf16x8 hb0 = ldsld<bf16x8>(rowa + ((kz ^ sw) << 4)), hb1 = ldsld<bf16x8>(rowa + (((4 + kz) ^ sw) << 4));
#pragma unroll
        for (int ob = 0; ob < 4; ++ob) {
            f32x4 o = {0.f, 0.f, 0.f, 0.f};
            o = MFMA16(ldsld<bf16x8>(wfa + (4 * ob) * STG_W), hb0, o);
            o = MFMA16(ldsld<bf16x8>(wfa + (4 * ob + 1) * STG_W), hb1, o);
            o = MFMA16(ldsld<bf16x8>(wfa + (4 * ob + 2) * STG_W), BS(zb[0]), o);
            o = MFMA16(ldsld<bf16x8>(wfa + (4 * ob + 3) * STG_W), BS(zb[1]), o);
            const uint32_t pa = rowa + (((2 * ob + (kz >> 1)) ^ sw) << 4) + 8 * (kz & 1);
            const u32x2 v = ldsld<u32x2>(pa);
            u32x2 r;
            r[0] = pkbf(bf2f(v[0] & 0xFFFF) + silu1(o[0]), bf2f(v[0] >> 16) + silu1(o[1]));
            r[1] = pkbf(bf2f(v[1] & 0xFFFF) + silu1(o[2]), bf2f(v[1] >> 16) + silu1(o[3]));
            ldsst<u32x2>(pa, r);
        }
#pragma unroll
        for (int q = 0; q < 2; ++q) {
            const int row = 8 * q + (lz >> 3);
            const u32x4 v = ldsld<u32x4>(stg + q * 1024 + lz * 16);
            if (row < nn && nb0 + row < nnodes) *(u32x4*)(out + (size_t)(node0 + nb0 + row) * 64 + (((lz & 7) ^ ((row >> 1) & 7)) << 3)) = v;
        }
        if (valid && kz == 0) {
            const f32x4 pd = ldsld<f32x4>(LB + L_P + nl * PROW + 256);
            uint16_t* po = out + (size_t)N * 64 + (size_t)ng * 3;
            po[0] = f2bf(pd[0] + 0.5f * Z2[0]); po[1] = f2bf(pd[1] + 0.5f * Z2[1]); po[2] = f2bf(pd[2] + 0.5f * Z2[2]);
        }
    }
    asm volatile(".Lcpf_z:");
}

static DI float rbf(float f) { return bf2f(f2bf(f)); }
__global__ void fb_zero(float* p, int n) { int i = blockIdx.x * 256 + threadIdx.x; if (i < n) p[i] = 0.f; }
__global__ void fb_edge(const uint16_t* h, const uint16_t* pos, const uint16_t* ea, const int* src, const int* dst,
                        const uint16_t* we, const uint16_t* wx, float* mi, float* pa, int E) {
    int e = blockIdx.x * 4 + (threadIdx.x >> 6);
    int f = threadIdx.x & 63;
    if (e >= E) return;
    size_t s = src[e], d = dst[e];
    float df0 = bf2f(pos[d * 3]) - bf2f(pos[s * 3]), df1 = bf2f(pos[d * 3 + 1]) - bf2f(pos[s * 3 + 1]), df2 = bf2f(pos[d * 3 + 2]) - bf2f(pos[s * 3 + 2]);
    float d2 = rbf(df0 * df0 + df1 * df1 + df2 * df2);
    float acc = 0.f;
    for (int k = 0; k < 64; ++k) acc += bf2f(h[d * 64 + k]) * bf2f(we[k * 64 + f]);
    for (int k = 0; k < 64; ++k) acc += bf2f(h[s * 64 + k]) * bf2f(we[(64 + k) * 64 + f]);
    acc += d2 * bf2f(we[128 * 64 + f]);
    for (int k = 0; k < 16; ++k) acc += bf2f(ea[(size_t)e * 16 + k]) * bf2f(we[(129 + k) * 64 + f]);
    float m = rbf(silu1(acc));
    float cs = m * bf2f(wx[f]);
    for (int o = 32; o > 0; o >>= 1) cs += __shfl_xor(cs, o, 64);
    atomicAdd(&mi[d * 64 + f], m);
    if (f < 3) atomicAdd(&pa[d * 3 + f], (f == 0 ? df0 : f == 1 ? df1 : df2) * cs);
}
__global__ void fb_node(const uint16_t* h, const uint16_t* pos, const uint16_t* wh, const float* mi, const float* pa, uint16_t* out, int N) {
    int n = blockIdx.x * 4 + (threadIdx.x >> 6);
    int f = threadIdx.x & 63;
    if (n >= N) return;
    float acc = 0.f;
    for (int k = 0; k < 64; ++k) acc += bf2f(h[(size_t)n * 64 + k]) * bf2f(wh[k * 64 + f]);
    for (int k = 0; k < 64; ++k) acc += rbf(mi[(size_t)n * 64 + k]) * bf2f(wh[(64 + k) * 64 + f]);
    out[(size_t)n * 64 + f] = f2bf(bf2f(h[(size_t)n * 64 + f]) + silu1(acc));
    if (f < 3) out[(size_t)N * 64 + (size_t)n * 3 + f] = f2bf(bf2f(pos[(size_t)n * 3 + f]) + pa[(size_t)n * 3 + f]);
}

extern "C" void kernel_launch(void* const* d_in, const int* in_sizes, int n_in,
                              void* d_out, int out_size, void* d_ws, size_t ws_size,
                              hipStream_t stream) {
    const uint16_t* h = (const uint16_t*)d_in[0];
    const uint16_t* pos = (const uint16_t*)d_in[1];
    const uint16_t* ea = (const uint16_t*)d_in[2];
    const int* src = (const int*)d_in[3];
    const int* dst = (const int*)d_in[4];
    const uint16_t* we = (const uint16_t*)d_in[5];
    const uint16_t* wx = (const uint16_t*)d_in[6];
    const uint16_t* wh = (const uint16_t*)d_in[7];
    const int N = in_sizes[0] / 64, E = in_sizes[3];
    const int nbin = (N + BINSZ - 1) / BINSZ, nblk = (E + CHUNK - 1) / CHUNK;
    if (nbin > NBIN || nblk > NBLK || N < 1 || E < 1) {
        float* mi = (float*)d_ws; float* pa = mi + (size_t)N * 64;
        int nz = N * 67;
        fb_zero<<<(nz + 255) / 256, 256, 0, stream>>>(mi, nz);
        if (E > 0) fb_edge<<<(E + 3) / 4, 256, 0, stream>>>(h, pos, ea, src, dst, we, wx, mi, pa, E);
        fb_node<<<(N + 3) / 4, 256, 0, stream>>>(h, pos, wh, mi, pa, (uint16_t*)d_out, N);
        return;
    }
    char* ws = (char*)d_ws;
    uint32_t* T = (uint32_t*)ws;
    u32x4* WF = (u32x4*)(ws + 262144);
    u32x2* pos4 = (u32x2*)(ws + 327680);
    u32x2* R = (u32x2*)(ws + 327680 + 802816);
    (void)hipFuncSetAttribute((const void*)k2_main, hipFuncAttributeMaxDynamicSharedMemorySize, L_TOTAL);
    k1_partition<<<nblk, 1024, 0, stream>>>(src, dst, (const u32x4*)h, pos, we, wx, wh, R, T, pos4, WF, N, E, (uint32_t)((NPH << 20) / N));
    k2_main<<<nbin, 1024, L_TOTAL, stream>>>(h, (const char*)ea, (const char*)pos4, R, T, WF, (uint16_t*)d_out, N, nblk);
}
```

```cpp
#include <hip/hip_runtime.h>
#include <hip/hip_bf16.h>
#include <stdint.h>

using u32x4 = __attribute__((ext_vector_type(4))) unsigned;
using u32x2 = __attribute__((ext_vector_type(2))) unsigned;
using f32x16 = __attribute__((ext_vector_type(16))) float;
using f32x4 = __attribute__((ext_vector_type(4))) float;
using f32x2 = __attribute__((ext_vector_type(2))) float;
using bf16x8 = __attribute__((ext_vector_type(8))) short;
using s16x4 = __attribute__((ext_vector_type(4))) short;
using bf16x2 = __attribute__((ext_vector_type(2))) __bf16;
#define DI __device__ __forceinline__
#define ACLOB "v120", "v121", "v122", "v123", "v124", "v125", "v126", "v127"
#define MFMA16(a, b, c) __builtin_amdgcn_mfma_f32_16x16x32_bf16((a), (b), (c), 0, 0, 0)
#define MFMA32(a, b, c) __builtin_amdgcn_mfma_f32_32x32x16_bf16((a), (b), (c), 0, 0, 0)
#define MFMA8(a, b, c) __builtin_amdgcn_mfma_f32_32x32x8bf16_1k((a), (b), (c), 0, 0, 0)
#define MFMA4(a, b, c) __builtin_amdgcn_mfma_f32_4x4x4bf16_1k((a), (b), (c), 0, 0, 0)
#define SB() __builtin_amdgcn_sched_barrier(0)
#define BS(x) __builtin_bit_cast(bf16x8, x)

constexpr int CHUNK = 3136;
constexpr int BINSZ = 196;
constexpr int NBIN = 256;
constexpr int NBLK = 256;
constexpr int CAP = 3392;
constexpr int NPH = 8;
constexpr int TRASH = CAP + 16 * 31 + 8;
constexpr int LISTN = TRASH + 136;

constexpr int NW = 16;
constexpr int PROW = 272;
constexpr int L_WL = 0;
constexpr int L_HIST = 12 * 1024;
constexpr int L_DL = L_HIST + 320 * 4;
constexpr int L_SRC = L_DL + 4096;
constexpr int L_W1 = L_SRC + LISTN * 2;
constexpr int L_P = L_W1 + LISTN * 4;
constexpr int L_STG = ((L_P + BINSZ * PROW + 127) / 128) * 128;
constexpr int STG_W = 4096;
constexpr int L_TOTAL = L_STG + NW * STG_W;
static_assert(L_TOTAL <= 163840, "LDS");
static_assert(L_W1 % 16 == 0 && L_DL % 16 == 0 && L_HIST % 16 == 0 && L_P % 16 == 0 && LISTN <= 4096 && (LISTN * 6) % 16 == 0, "align");
constexpr float NLOG2E = -1.44269504089f;

DI float bf2f(uint32_t v) { return __uint_as_float(v << 16); }
DI uint32_t pkbf(float a, float b) { bf16x2 v; v[0] = (__bf16)a; v[1] = (__bf16)b; return __builtin_bit_cast(uint32_t, v); }
DI uint16_t f2bf(float a) { __bf16 v = (__bf16)a; return __builtin_bit_cast(uint16_t, v); }
DI float silu1(float x) { return x * __builtin_amdgcn_rcpf(1.f + __builtin_amdgcn_exp2f(x * NLOG2E)); }

DI uint32_t wave_scan(uint32_t x) {
    x += (uint32_t)__builtin_amdgcn_update_dpp(0, (int)x, 0x111, 0xF, 0xF, false);
    x += (uint32_t)__builtin_amdgcn_update_dpp(0, (int)x, 0x112, 0xF, 0xF, false);
    x += (uint32_t)__builtin_amdgcn_update_dpp(0, (int)x, 0x114, 0xF, 0xF, false);
    x += (uint32_t)__builtin_amdgcn_update_dpp(0, (int)x, 0x118, 0xF, 0xF, false);
    x += (uint32_t)__builtin_amdgcn_update_dpp(0, (int)x, 0x142, 0xA, 0xF, false);
    x += (uint32_t)__builtin_amdgcn_update_dpp(0, (int)x, 0x143, 0xC, 0xF, false);
    return x;
}

DI const uint16_t* wf_src(int f, int l, int j, const uint16_t* we, const uint16_t* wx, const uint16_t* wh, bool& ok) {
    const int r = l & 31, h = l >> 5;
    const int kn = 8 * h + j, kp = 8 * (j >> 2) + 4 * h + (j & 3);
    ok = true;
    if (f < 8) return we + (64 + 16 * (f & 3) + kn) * 64 + 32 * (f >> 2) + r;
    if (f < 10) return we + (129 + kn) * 64 + 32 * (f - 8) + r;
    if (f == 10) { ok = h == 0 && (j & 3) == 0; return we + 128 * 64 + 32 * (j >> 2) + r; }
    if (f == 11) { const int q = (l >> 1) & 3; ok = l < 8; return wx + 32 * (q >> 1) + 16 * (q & 1) + 8 * (j >> 2) + 4 * (l & 1) + (j & 3); }
    if (f < 16) { ok = false; return wx; }
    if (f < 24) return we + (16 * ((f - 16) & 3) + kn) * 64 + 32 * ((f - 16) >> 2) + r;
    const int ob = (f - 24) >> 2, sx = (f - 24) & 3, kg = l >> 4, o = 16 * ob + (l & 15);
    if (sx < 2) return wh + (32 * sx + 8 * kg + j) * 64 + o;
    return wh + (64 + 32 * (sx - 2) + 16 * (j >> 2) + 4 * kg + (j & 3)) * 64 + o;
}

__global__ __launch_bounds__(1024) void k1_partition(const int* __restrict__ src, const int* __restrict__ dst,
                                                     const uint16_t* __restrict__ pos, const uint16_t* __restrict__ we, const uint16_t* __restrict__ wx,
                                                     const uint16_t* __restrict__ wh,
                                                     u32x2* __restrict__ R, uint32_t* __restrict__ T,
                                                     u32x2* __restrict__ pos4, u32x4* __restrict__ WF, int N, int E, uint32_t phmul) {
    __shared__ __attribute__((aligned(16))) uint32_t hist[NBIN];
    __shared__ __attribute__((aligned(16))) u32x2 recS[CHUNK];
    const int tid = threadIdx.x, blk = blockIdx.x;
    const int e0 = blk * CHUNK;
    const int cnt = min(CHUNK, E - e0);
    if (tid < NBIN) hist[tid] = 0;
    int d[4], s[4];
#pragma unroll
    for (int r = 0; r < 4; ++r) { int i = r * 1024 + tid; d[r] = i < cnt ? dst[e0 + i] : 0; s[r] = i < cnt ? src[e0 + i] : 0; }
    unsigned cpf = 0;
    {
        unsigned long long pc; unsigned sz;
        asm volatile("s_getpc_b64 %0\n.Lcpf_a:\n\ts_mov_b32 %1, .Lcpf_z-.Lcpf_a" : "=s"(pc), "=s"(sz));
        if (blk < 16 && sz < 65536u && (unsigned)tid * 64u + 4u <= sz) cpf = *(const __attribute__((address_space(1))) unsigned*)(pc + (unsigned)tid * 64u);
    }
    for (int f = blk; f < 40; f += gridDim.x) {
        if (tid < 512) {
            bool ok;
            const uint16_t* p = wf_src(f, tid >> 3, tid & 7, we, wx, wh, ok);
            const uint16_t v = *p;
            ((uint16_t*)WF)[f * 512 + tid] = ok ? v : (uint16_t)0;
        }
    }
    for (int n = blk * BINSZ + tid - 512; tid >= 512 && tid < 512 + BINSZ && n < N; n += gridDim.x * BINSZ) {
        u32x2 p; p[0] = pos[n * 3] | ((uint32_t)pos[n * 3 + 1] << 16); p[1] = pos[n * 3 + 2]; pos4[n] = p;
    }
    __syncthreads();
    uint32_t bin[4], rank[4];
#pragma unroll
    for (int r = 0; r < 4; ++r) {
        int i = r * 1024 + tid;
        bin[r] = (uint32_t)d[r] / BINSZ;
        rank[r] = 0;
        if (i < cnt) rank[r] = atomicAdd(&hist[bin[r]], 1u);
    }
    __syncthreads();
    if (tid < 64) {
        u32x4 hv = *(const u32x4*)(hist + 4 * tid);
        const uint32_t sum = hv[0] + hv[1] + hv[2] + hv[3], x = wave_scan(sum);
        u32x4 st; st[0] = x - sum; st[1] = st[0] + hv[0]; st[2] = st[1] + hv[1]; st[3] = st[2] + hv[2];
        *(u32x4*)(hist + 4 * tid) = st;
#pragma unroll
        for (int i = 0; i < 4; ++i) T[(4 * tid + i) * NBLK + blk] = st[i] | (hv[i] << 16);
    }
    __syncthreads();
#pragma unroll
    for (int r = 0; r < 4; ++r) {
        int i = r * 1024 + tid;
        if (i < cnt) {
            const uint32_t dl = d[r] - bin[r] * BINSZ;
            const uint32_t g5 = dl / 61, r5 = dl - 61 * g5;
            const uint32_t key = (5 * g5 + (r5 < 13 ? 0u : (r5 - 1) / 12)) * NPH + min(((uint32_t)s[r] * phmul) >> 20, (uint32_t)(NPH - 1));
            u32x2 rec; rec[0] = (uint32_t)(e0 + i) | (dl << 24); rec[1] = (uint32_t)s[r] | (key << 18);
            recS[hist[bin[r]] + rank[r]] = rec;
        }
    }
    __syncthreads();
#pragma unroll
    for (int r = 0; r < 2; ++r) {
        int i = r * 1024 + tid;
        if (2 * i < cnt) *(u32x4*)(R + e0 + 2 * i) = *(const u32x4*)(recS + 2 * i);
    }
    asm volatile("" :: "v"(cpf));
}

DI void dma16(uint32_t ldsaddr, uint32_t voff, const void* base) {
    asm volatile("s_mov_b32 m0, %0\n\ts_nop 0\n\tglobal_load_lds_dwordx4 %1, %2" :: "s"(ldsaddr), "v"(voff), "s"(base) : "memory");
}
typedef __attribute__((address_space(3))) s16x4* lds_s16x4_p;
typedef __attribute__((address_space(3))) uint32_t* lds_u32_p;
DI s16x4 trread(uint32_t addr) { return __builtin_amdgcn_ds_read_tr16_b64_v4i16((lds_s16x4_p)(uintptr_t)addr); }
template <class T> DI T ldsld(uint32_t addr) { return *(__attribute__((address_space(3))) T*)(uintptr_t)addr; }
template <class T> DI void ldsst(uint32_t addr, T v) { *(__attribute__((address_space(3))) T*)(uintptr_t)addr = v; }
template <int V> struct IC { static constexpr int value = V; };

__global__ __launch_bounds__(1024, 4) __attribute__((amdgpu_num_vgpr(60)))
void k2_main(const uint16_t* __restrict__ hg, const char* __restrict__ eag, const char* __restrict__ pos4g,
             const u32x2* __restrict__ R, const uint32_t* __restrict__ T,
             const u32x4* __restrict__ WF, uint16_t* __restrict__ out, int N, int nblk) {
    extern __shared__ __attribute__((aligned(16))) char ldsraw[];
    const uint32_t LB = (uint32_t)(uintptr_t)(__attribute__((address_space(3))) char*)ldsraw;
    const int tid = threadIdx.x, lane = tid & 63;
    const int w = __builtin_amdgcn_readfirstlane(tid >> 6);
    const int c = blockIdx.x;
    const int node0 = c * BINSZ;
    const int nnodes = min(BINSZ, N - node0);
    const uint32_t stg = LB + L_STG + w * STG_W;
    if (w >= 4) dma16(LB + L_WL + (w - 4) * 1024, (uint32_t)((w - 4) * 1024 + lane * 16), WF);
    if (w < 8) dma16(stg + 2048, (uint32_t)((16 + w) * 1024 + lane * 16), WF);
    u32x4 T4 = *(const u32x4*)(T + (size_t)c * NBLK + 4 * lane);
    if (tid < 320) ldsst<uint32_t>(LB + L_HIST + tid * 4, 0u);
    ldsst<uint32_t>(LB + L_DL + tid * 4, 0xFFFFFFFFu);
    for (int i = tid; i < LISTN * 6 / 16; i += 1024) ldsst<u32x4>(LB + L_SRC + i * 16, u32x4{0u, 0u, 0u, 0u});
    if (w < 13) {
#pragma unroll
        for (int q = 0; q < 2; ++q) {
            const int row = 8 * q + (lane >> 3);
            const int ng = min(node0 + 16 * w + row, N - 1);
            dma16(stg + q * 1024, (uint32_t)(ng << 7) | (uint32_t)(((lane & 7) ^ ((row >> 1) & 7)) << 4), hg);
        }
    }
    if (tid < BINSZ) {
        const u32x2 pv = *(const u32x2*)(pos4g + (size_t)min(node0 + tid, N - 1) * 8);
        f32x4 pd0; pd0[0] = bf2f(pv[0] & 0xFFFF); pd0[1] = bf2f(pv[0] >> 16); pd0[2] = bf2f(pv[1] & 0xFFFF); pd0[3] = 0.f;
        ldsst<f32x4>(LB + L_P + tid * PROW + 256, pd0);
    }
    f32x4 Z[4], Z2;
#pragma unroll
    for (int i = 0; i < 4; ++i) { Z[0][i] = 0.f; Z[1][i] = 0.f; Z[2][i] = 0.f; Z[3][i] = 0.f; Z2[i] = 0.f; }
    const int nb0 = 12 * w + (w + 4) / 5, nb1 = 12 * (w + 1) + (w + 5) / 5;
    asm volatile("v_mov_b32 v124, 0\n\tv_mov_b32 v125, 0" ::: ACLOB);

    int seg0 = 0, round = 0;
    bool first = true, wdone = false;
    for (;;) {
        int lq = __builtin_amdgcn_mbcnt_hi(~0u, __builtin_amdgcn_mbcnt_lo(~0u, 0u)); asm volatile("" : "+v"(lq));
        const int eq = lq & 31, hq = lq >> 5;
        if (!first) {
            T4 = *(const u32x4*)(T + (size_t)c * NBLK + 4 * lq);
            __syncthreads();
            if (w * 64 + lq < 320) ldsst<uint32_t>(LB + L_HIST + (w * 64 + lq) * 4, 0u);
            ldsst<uint32_t>(LB + L_DL + (w * 64 + lq) * 4, 0xFFFFFFFFu);
            for (int i = w * 64 + lq; i < LISTN * 6 / 16; i += 1024) ldsst<u32x4>(LB + L_SRC + i * 16, u32x4{0u, 0u, 0u, 0u});
        }
        bool more = false;
        uint32_t sum = 0;
#pragma unroll
        for (int i = 0; i < 4; ++i) {
            const int sg = 4 * lq + i;
            int cn = (int)(T4[i] >> 16) - 32 * round;
            if (sg >= nblk) cn = 0;
            more = more || cn > 32;
            cn = min(max(cn, 0), 32);
            if (sg < seg0) cn = 0;
            T4[i] = cn > 0 ? (((T4[i] & 0xFFFF) + 32 * round) | ((uint32_t)cn << 16)) : 0u;
            sum += cn;
        }
        u32x4 ra[2], rb[2];
        int cn4[2];
        const int L = w * 4 + (lq >> 4), odd = (lq >> 3) & 1;
#pragma unroll
        for (int p = 0; p < 2; ++p) {
            const uint32_t t0 = __shfl(T4[p], L, 64), t1 = __shfl(T4[2 + p], L, 64);
            const uint32_t tv = odd ? t1 : t0;
            cn4[p] = (int)(tv >> 16) - 4 * (lq & 7);
            const u32x2* rp = R + (4 * L + 2 * odd + p) * CHUNK + (tv & 0xFFFF) + (cn4[p] > 0 ? 4 * (lq & 7) : 0);
            ra[p] = *(const u32x4*)rp; rb[p] = *(const u32x4*)(rp + 2);
        }
        const uint32_t xs = wave_scan(sum);
        uint32_t n = __builtin_amdgcn_readlane(xs, 63);
        int seg1 = NBLK;
        if (n > (uint32_t)CAP) {
            const int lcut = __popcll(__ballot(xs <= (uint32_t)CAP));
            n = __builtin_amdgcn_readlane(xs, lcut - 1);
            seg1 = 4 * lcut;
            if (L >= lcut) { cn4[0] = 0; cn4[1] = 0; }
        }
        const bool anymore = __any(more);
        T4 = u32x4{0u, 0u, 0u, 0u};
        asm volatile("s_waitcnt vmcnt(4) lgkmcnt(0)" ::: "memory");
        __builtin_amdgcn_s_barrier();
        if (first && w < 14) {
            f32x16 p0;
#pragma unroll
            for (int i = 0; i < 16; ++i) p0[i] = 0.f;
            const uint32_t wa = LB + L_STG + (w & 1) * 4 * STG_W + 2048 + lq * 16;
            const uint32_t ha0 = LB + L_STG + min((w & ~1) + (eq >> 4), 12) * STG_W + (eq & 15) * 128 + ((hq ^ ((eq >> 1) & 7)) << 4);
#pragma unroll
            for (int s = 0; s < 4; ++s) p0 = MFMA32(ldsld<bf16x8>(wa + s * STG_W), ldsld<bf16x8>(ha0 ^ (32 * s)), p0);
            const int nl = 32 * (w >> 1) + eq;
            if (nl < BINSZ) {
                const uint32_t pa = LB + L_P + nl * PROW + 16 * hq + 128 * (w & 1);
#pragma unroll
                for (int g = 0; g < 4; ++g) {
                    f32x4 a;
#pragma unroll
                    for (int i = 0; i < 4; ++i) a[i] = p0[4 * g + i];
                    ldsst<f32x4>(pa + 32 * g, a);
                }
            }
        }
        uint32_t ha[8], rk[8];
        const uint32_t dummy = 1024 + 4 * lq;
#pragma unroll
        for (int p = 0; p < 2; ++p) {
#pragma unroll
            for (int i = 0; i < 4; ++i) {
                const uint32_t w1v = i == 0 ? ra[p][1] : i == 1 ? ra[p][3] : i == 2 ? rb[p][1] : rb[p][3];
                ha[4 * p + i] = cn4[p] > i ? (w1v >> 16) : dummy;
                rk[4 * p + i] = __hip_atomic_fetch_add((lds_u32_p)(uintptr_t)(LB + L_HIST + ha[4 * p + i]), 1u, __ATOMIC_RELAXED, __HIP_MEMORY_SCOPE_WORKGROUP);
            }
        }
        __syncthreads();
        uint32_t eb, cntu;
        {
            const u32x4 hv = ldsld<u32x4>(LB + L_HIST + lq * 16);
            const uint32_t sm = hv[0] + hv[1] + hv[2] + hv[3];
            uint32_t tw = sm;
            if (NPH >= 8) tw += (uint32_t)__builtin_amdgcn_update_dpp(0, (int)tw, 0xB1, 0xF, 0xF, false);
            if (NPH >= 16) tw += (uint32_t)__builtin_amdgcn_update_dpp(0, (int)tw, 0x4E, 0xF, 0xF, false);
            const uint32_t sp = sm + ((lq & (NPH / 4 - 1)) == NPH / 4 - 1 ? ((0u - tw) & 31u) : 0u);
            const uint32_t x = wave_scan(sp) - sp;
            u32x4 st; st[0] = x; st[1] = st[0] + hv[0]; st[2] = st[1] + hv[1]; st[3] = st[2] + hv[2];
            ldsst<u32x4>(stg + lq * 16, st);
            ldsst<uint32_t>(stg + dummy, (uint32_t)TRASH);
            eb = __builtin_amdgcn_readlane(x, (NPH / 4) * w); cntu = __builtin_amdgcn_readlane(tw, (NPH / 4) * w);
        }
#pragma unroll
        for (int k = 0; k < 8; ++k) rk[k] += ldsld<uint32_t>(stg + ha[k]);
#pragma unroll
        for (int p = 0; p < 2; ++p) {
#pragma unroll
            for (int i = 0; i < 4; ++i) {
                const uint32_t w0v = i == 0 ? ra[p][0] : i == 1 ? ra[p][2] : i == 2 ? rb[p][0] : rb[p][2];
                const uint32_t w1v = i == 0 ? ra[p][1] : i == 1 ? ra[p][3] : i == 2 ? rb[p][1] : rb[p][3];
                ldsst<uint16_t>(LB + L_SRC + rk[4 * p + i] * 2, (uint16_t)w1v);
                ldsst<uint32_t>(LB + L_W1 + rk[4 * p + i] * 4, w0v);
                ldsst<uint8_t>(LB + L_DL + rk[4 * p + i], (uint8_t)(w0v >> 24));
            }
        }
        __syncthreads();
        first = false;
        const bool last = seg1 == NBLK && !anymore;
        if (seg1 == NBLK) { seg0 = 0; ++round; } else seg0 = seg1;
        const int cq = lq & 15, kg = lq >> 4;
        const int nt = (int)(cntu + 31) >> 5;
        const uint32_t nrep = (uint32_t)(nb0 + cq) * 0x01010101u;
        uint32_t dla = LB + L_DL + eb + 8 * kg;
        if (nt == 0) { if (last) break; continue; }

        const uint32_t hfa = stg + eq * 128 + ((hq ^ ((eq >> 1) & 7)) << 4);
        const uint32_t lp0 = ((lq & 7) ^ ((lq >> 4) & 7)) << 4, lp1 = lp0 ^ 64;
        const uint32_t mwa = stg + eq * 72 + 8 * hq;
        const uint32_t tra = stg + (8 * kg + (cq >> 2)) * 72 + (cq & 3) * 8;
        const uint32_t vwa = stg + 2304 + lq * 8;
        const uint32_t vra = stg + 2304 + (8 * kg + (cq >> 2)) * 8;
        const uint32_t wla = LB + L_WL + lq * 16, wxa = LB + L_WL + 11 * 1024 + hq * 16;

        uint32_t w1c;
        uint32_t cse = LB + L_SRC + 2 * (eb + eq), cw1 = LB + L_W1 + 4 * (eb + eq), cs8 = LB + L_SRC + 2 * (eb + (lq >> 3));
        auto pf_small = [&]() {
            const uint32_t ixm = ldsld<uint16_t>(cse);
            w1c = ldsld<uint32_t>(cw1);
            cse += 64; cw1 += 128;
            const uint32_t eo = ((w1c << 5) & 0x1FFFFFE0u) | (16 * hq), po = ixm << 3;
            const char* b0 = eag; const char* b1 = pos4g;
            asm volatile("global_load_dwordx4 v[120:123], %0, %2\n\ts_mov_b32 exec_hi, 0\n\tglobal_load_dwordx2 v[124:125], %1, %3\n\ts_mov_b32 exec_hi, -1"
                         :: "v"(eo), "v"(po), "s"(b0), "s"(b1) : "memory", ACLOB);
        };
        uint32_t ix0, ix1, ix2, ix3;
        auto pf_idx = [&]() {
            ix0 = ldsld<uint16_t>(cs8); ix1 = ldsld<uint16_t>(cs8 + 16); ix2 = ldsld<uint16_t>(cs8 + 32); ix3 = ldsld<uint16_t>(cs8 + 48);
            cs8 += 64;
        };
        auto pf_dma = [&]() {
            dma16(stg, (ix0 << 7) | lp0, hg); dma16(stg + 1024, (ix1 << 7) | lp1, hg);
            dma16(stg + 2048, (ix2 << 7) | lp0, hg); dma16(stg + 3072, (ix3 << 7) | lp1, hg);
        };

        pf_idx();
        pf_dma();
        pf_small();
        const int th1 = (nt + 3) >> 2, th2 = (nt + 1) >> 1, th3 = (3 * nt + 3) >> 2;
        __builtin_amdgcn_s_setprio(3);
#pragma unroll 1
        for (int t = 0; t < nt; ++t) {
            if (t == th1) __builtin_amdgcn_s_setprio(2);
            if (t == th2) __builtin_amdgcn_s_setprio(1);
            if (t == th3) __builtin_amdgcn_s_setprio(0);
            asm volatile("s_waitcnt vmcnt(0)" ::: "memory");
            const uint32_t prow = LB + L_P + (w1c >> 24) * PROW;
            const uint32_t pac = prow + 16 * hq;
            const f32x4 pd = ldsld<f32x4>(prow + 256);
            bf16x8 hf[4];
#pragma unroll
            for (int s = 0; s < 4; ++s) hf[s] = ldsld<bf16x8>(hfa ^ (32 * s));
            float q0, q1, q2;
            asm volatile("v_lshlrev_b32 %0, 16, v124\n\tv_and_b32 %1, 0xffff0000, v124\n\tv_lshlrev_b32 %2, 16, v125" : "=&v"(q0), "=&v"(q1), "=&v"(q2));
            const float dx = pd[0] - q0, dy = pd[1] - q1, dz = pd[2] - q2;
            const float d2 = dx * dx + dy * dy + dz * dz;
            u32x2 dv; dv[0] = pkbf(d2, d2); dv[1] = dv[0];
            const s16x4 d2v = __builtin_bit_cast(s16x4, dv);
            u32x4 S;
            {
                const u32x2 dv8 = ldsld<u32x2>(dla);
                dla += 32;
#pragma unroll
                for (int i = 0; i < 2; ++i) {
                    const uint32_t tt = dv8[i] ^ nrep;
                    const uint32_t z = (~(((tt & 0x7F7F7F7Fu) + 0x7F7F7F7Fu) | tt) & 0x80808080u) >> 1;
                    S[2 * i] = __builtin_amdgcn_perm(0u, z, 0x010C000Cu); S[2 * i + 1] = __builtin_amdgcn_perm(0u, z, 0x030C020Cu);
                }
            }
            f32x4 cs = {0.f, 0.f, 0.f, 0.f};
#pragma unroll
            for (int fb = 0; fb < 2; ++fb) {
                f32x16 x;
#pragma unroll
                for (int g = 0; g < 4; ++g) {
                    const f32x4 v = ldsld<f32x4>(pac + 128 * fb + 32 * g);
#pragma unroll
                    for (int i = 0; i < 4; ++i) x[4 * g + i] = v[i];
                }
                { const bf16x8 wa = ldsld<bf16x8>(wla + (8 + fb) * 1024);
                  asm volatile("v_mfma_f32_32x32x16_bf16 %0, %1, v[120:123], %0" : "+v"(x) : "v"(wa)); }
                if (fb == 1) { if (t + 1 < nt) { pf_small(); pf_idx(); } else if (last) { dma16(stg + 3072, (uint32_t)((24 + w) * 1024 + lq * 16), WF); wdone = true; } }
                x = MFMA8(ldsld<s16x4>(wla + 10 * 1024 + 8 * fb), d2v, x);
#pragma unroll
                for (int s = 0; s < 4; ++s) x = MFMA32(ldsld<bf16x8>(wla + (4 * fb + s) * 1024), hf[s], x);
                uint32_t m[8];
#pragma unroll
                for (int b = 0; b < 4; ++b) {
                    float t0 = x[4 * b] * NLOG2E, t1 = x[4 * b + 1] * NLOG2E, t2 = x[4 * b + 2] * NLOG2E, t3 = x[4 * b + 3] * NLOG2E;
                    t0 = __builtin_amdgcn_exp2f(t0); t1 = __builtin_amdgcn_exp2f(t1); t2 = __builtin_amdgcn_exp2f(t2); t3 = __builtin_amdgcn_exp2f(t3);
                    t0 += 1.f; t1 += 1.f; t2 += 1.f; t3 += 1.f;
                    t0 = __builtin_amdgcn_rcpf(t0); t1 = __builtin_amdgcn_rcpf(t1); t2 = __builtin_amdgcn_rcpf(t2); t3 = __builtin_amdgcn_rcpf(t3);
                    m[2 * b] = pkbf(t0 * x[4 * b], t1 * x[4 * b + 1]); m[2 * b + 1] = pkbf(t2 * x[4 * b + 2], t3 * x[4 * b + 3]);
                }
#pragma unroll
                for (int g = 0; g < 4; ++g) { u32x2 v; v[0] = m[2 * g]; v[1] = m[2 * g + 1]; ldsst<u32x2>(mwa + 16 * g, v); }
#pragma unroll
                for (int q = 0; q < 2; ++q) {
                    const bf16x8 wv = ldsld<bf16x8>(wxa + (2 * fb + q) * 32);
                    u32x2 b0, b1; b0[0] = m[4 * q]; b0[1] = m[4 * q + 1]; b1[0] = m[4 * q + 2]; b1[1] = m[4 * q + 3];
                    cs = MFMA4(__builtin_shufflevector(wv, wv, 0, 1, 2, 3), __builtin_bit_cast(s16x4, b0), cs);
                    cs = MFMA4(__builtin_shufflevector(wv, wv, 4, 5, 6, 7), __builtin_bit_cast(s16x4, b1), cs);
                }
#pragma unroll
                for (int u = 0; u < 2; ++u) {
                    const s16x4 a0 = trread(tra + 32 * u), a1 = trread(tra + 32 * u + 288);
                    Z[2 * fb + u] = MFMA16(__builtin_shufflevector(a0, a1, 0, 1, 2, 3, 4, 5, 6, 7), BS(S), Z[2 * fb + u]);
                }
            }
            {
                const uint32_t c0 = __float_as_uint(cs[0]);
                const auto sw = __builtin_amdgcn_permlane32_swap(c0, c0, false, false);
                const float cv = __uint_as_float(sw[0]) + __uint_as_float(sw[1]);
                u32x2 vv; vv[0] = pkbf(dx * cv, dy * cv); vv[1] = pkbf(dz * cv, dz);
                ldsst<u32x2>(vwa, vv);
                const s16x4 a0 = trread(vra), a1 = trread(vra + 32);
                asm volatile("s_waitcnt lgkmcnt(0)" ::: "memory");
                if (t + 1 < nt) pf_dma();
                Z2 = MFMA16(__builtin_shufflevector(a0, a1, 0, 1, 2, 3, 4, 5, 6, 7), BS(S), Z2);
            }
        }
        __builtin_amdgcn_s_setprio(0);
        if (last) break;
    }
    {
        int lz = __builtin_amdgcn_mbcnt_hi(~0u, __builtin_amdgcn_mbcnt_lo(~0u, 0u)); asm volatile("" : "+v"(lz));
        const int cz = lz & 15, kz = lz >> 4;
        const int nn = nb1 - nb0;
        const int nl = nb0 + cz;
        const bool valid = cz < nn && nl < nnodes;
        const int ng = min(node0 + nl, N - 1);
        asm volatile("s_waitcnt lgkmcnt(0)" ::: "memory");
        if (!wdone) dma16(stg + 3072, (uint32_t)((24 + w) * 1024 + lz * 16), WF);
#pragma unroll
        for (int q = 0; q < 2; ++q) {
            const int row = 8 * q + (lz >> 3);
            const int nr = min(node0 + nb0 + row, N - 1);
            dma16(stg + q * 1024, (uint32_t)(nr << 7) | (uint32_t)(((lz & 7) ^ ((row >> 1) & 7)) << 4), hg);
        }
        u32x4 zb[2];
#pragma unroll
        for (int s = 0; s < 2; ++s) {
            zb[s][0] = pkbf(Z[2 * s][0] * 0.5f, Z[2 * s][1] * 0.5f); zb[s][1] = pkbf(Z[2 * s][2] * 0.5f, Z[2 * s][3] * 0.5f);
            zb[s][2] = pkbf(Z[2 * s + 1][0] * 0.5f, Z[2 * s + 1][1] * 0.5f); zb[s][3] = pkbf(Z[2 * s + 1][2] * 0.5f, Z[2 * s + 1][3] * 0.5f);
        }
        asm volatile("s_waitcnt vmcnt(0)" ::: "memory");
        __builtin_amdgcn_s_barrier();
        const uint32_t wfa = LB + L_STG + 3072 + lz * 16;
        const uint32_t sw = (cz >> 1) & 7, rowa = stg + cz * 128;
        const bf16x8 hb0 = ldsld<bf16x8>(rowa + ((kz ^ sw) << 4)), hb1 = ldsld<bf16x8>(rowa + (((4 + kz) ^ sw) << 4));
#pragma unroll
        for (int ob = 0; ob < 4; ++ob) {
            f32x4 o = {0.f, 0.f, 0.f, 0.f};
            o = MFMA16(ldsld<bf16x8>(wfa + (4 * ob) * STG_W), hb0, o);
            o = MFMA16(ldsld<bf16x8>(wfa + (4 * ob + 1) * STG_W), hb1, o);
            o = MFMA16(ldsld<bf16x8>(wfa + (4 * ob + 2) * STG_W), BS(zb[0]), o);
            o = MFMA16(ldsld<bf16x8>(wfa + (4 * ob + 3) * STG_W), BS(zb[1]), o);
            const uint32_t pa = rowa + (((2 * ob + (kz >> 1)) ^ sw) << 4) + 8 * (kz & 1);
            const u32x2 v = ldsld<u32x2>(pa);
            u32x2 r;
            r[0] = pkbf(bf2f(v[0] & 0xFFFF) + silu1(o[0]), bf2f(v[0] >> 16) + silu1(o[1]));
            r[1] = pkbf(bf2f(v[1] & 0xFFFF) + silu1(o[2]), bf2f(v[1] >> 16) + silu1(o[3]));
            ldsst<u32x2>(pa, r);
        }
#pragma unroll
        for (int q = 0; q < 2; ++q) {
            const int row = 8 * q + (lz >> 3);
            const u32x4 v = ldsld<u32x4>(stg + q * 1024 + lz * 16);
            if (row < nn && nb0 + row < nnodes) *(u32x4*)(out + (size_t)(node0 + nb0 + row) * 64 + (((lz & 7) ^ ((row >> 1) & 7)) << 3)) = v;
        }
        if (valid && kz == 0) {
            const f32x4 pd = ldsld<f32x4>(LB + L_P + nl * PROW + 256);
            uint16_t* po = out + (size_t)N * 64 + (size_t)ng * 3;
            po[0] = f2bf(pd[0] + 0.5f * Z2[0]); po[1] = f2bf(pd[1] + 0.5f * Z2[1]); po[2] = f2bf(pd[2] + 0.5f * Z2[2]);
        }
    }
    asm volatile(".Lcpf_z:");
}

static DI float rbf(float f) { return bf2f(f2bf(f)); }
__global__ void fb_zero(float* p, int n) { int i = blockIdx.x * 256 + threadIdx.x; if (i < n) p[i] = 0.f; }
__global__ void fb_edge(const uint16_t* h, const uint16_t* pos, const uint16_t* ea, const int* src, const int* dst,
                        const uint16_t* we, const uint16_t* wx, float* mi, float* pa, int E) {
    int e = blockIdx.x * 4 + (threadIdx.x >> 6);
    int f = threadIdx.x & 63;
    if (e >= E) return;
    size_t s = src[e], d = dst[e];
    float df0 = bf2f(pos[d * 3]) - bf2f(pos[s * 3]), df1 = bf2f(pos[d * 3 + 1]) - bf2f(pos[s * 3 + 1]), df2 = bf2f(pos[d * 3 + 2]) - bf2f(pos[s * 3 + 2]);
    float d2 = rbf(df0 * df0 + df1 * df1 + df2 * df2);
    float acc = 0.f;
    for (int k = 0; k < 64; ++k) acc += bf2f(h[d * 64 + k]) * bf2f(we[k * 64 + f]);
    for (int k = 0; k < 64; ++k) acc += bf2f(h[s * 64 + k]) * bf2f(we[(64 + k) * 64 + f]);
    acc += d2 * bf2f(we[128 * 64 + f]);
    for (int k = 0; k < 16; ++k) acc += bf2f(ea[(size_t)e * 16 + k]) * bf2f(we[(129 + k) * 64 + f]);
    float m = rbf(silu1(acc));
    float cs = m * bf2f(wx[f]);
    for (int o = 32; o > 0; o >>= 1) cs += __shfl_xor(cs, o, 64);
    atomicAdd(&mi[d * 64 + f], m);
    if (f < 3) atomicAdd(&pa[d * 3 + f], (f == 0 ? df0 : f == 1 ? df1 : df2) * cs);
}
__global__ void fb_node(const uint16_t* h, const uint16_t* pos, const uint16_t* wh, const float* mi, const float* pa, uint16_t* out, int N) {
    int n = blockIdx.x * 4 + (threadIdx.x >> 6);
    int f = threadIdx.x & 63;
    if (n >= N) return;
    float acc = 0.f;
    for (int k = 0; k < 64; ++k) acc += bf2f(h[(size_t)n * 64 + k]) * bf2f(wh[k * 64 + f]);
    for (int k = 0; k < 64; ++k) acc += rbf(mi[(size_t)n * 64 + k]) * bf2f(wh[(64 + k) * 64 + f]);
    out[(size_t)n * 64 + f] = f2bf(bf2f(h[(size_t)n * 64 + f]) + silu1(acc));
    if (f < 3) out[(size_t)N * 64 + (size_t)n * 3 + f] = f2bf(bf2f(pos[(size_t)n * 3 + f]) + pa[(size_t)n * 3 + f]);
}

extern "C" void kernel_launch(void* const* d_in, const int* in_sizes, int n_in,
                              void* d_out, int out_size, void* d_ws, size_t ws_size,
                              hipStream_t stream) {
    const uint16_t* h = (const uint16_t*)d_in[0];
    const uint16_t* pos = (const uint16_t*)d_in[1];
    const uint16_t* ea = (const uint16_t*)d_in[2];
    const int* src = (const int*)d_in[3];
    const int* dst = (const int*)d_in[4];
    const uint16_t* we = (const uint16_t*)d_in[5];
    const uint16_t* wx = (const uint16_t*)d_in[6];
    const uint16_t* wh = (const uint16_t*)d_in[7];
    const int N = in_sizes[0] / 64, E = in_sizes[3];
    const int nbin = (N + BINSZ - 1) / BINSZ, nblk = (E + CHUNK - 1) / CHUNK;
    if (nbin > NBIN || nblk > NBLK || N < 1 || E < 1) {
        float* mi = (float*)d_ws; float* pa = mi + (size_t)N * 64;
        int nz = N * 67;
        fb_zero<<<(nz + 255) / 256, 256, 0, stream>>>(mi, nz);
        if (E > 0) fb_edge<<<(E + 3) / 4, 256, 0, stream>>>(h, pos, ea, src, dst, we, wx, mi, pa, E);
        fb_node<<<(N + 3) / 4, 256, 0, stream>>>(h, pos, wh, mi, pa, (uint16_t*)d_out, N);
        return;
    }
    char* ws = (char*)d_ws;
    uint32_t* T = (uint32_t*)ws;
    u32x4* WF = (u32x4*)(ws + 262144);
    u32x2* pos4 = (u32x2*)(ws + 327680);
    u32x2* R = (u32x2*)(ws + 327680 + 802816);
    (void)hipFuncSetAttribute((const void*)k2_main, hipFuncAttributeMaxDynamicSharedMemorySize, L_TOTAL);
    k1_partition<<<nblk, 1024, 0, stream>>>(src, dst, pos, we, wx, wh, R, T, pos4, WF, N, E, (uint32_t)((NPH << 20) / N));
    k2_main<<<nbin, 1024, L_TOTAL, stream>>>(h, (const char*)ea, (const char*)pos4, R, T, WF, (uint16_t*)d_out, N, nblk);
}
```
